```python
import torch

def _softmax(x, axis=-1):
    return torch.softmax(x, dim=axis)
def _gelu(x):
    return torch.nn.functional.gelu(x, approximate="tanh")
def _silu(x):
    return torch.nn.functional.silu(x)
def _sigmoid(x):
    return torch.sigmoid(x)
def _layernorm(x, axis=-1, eps=1e-5):
    m = x.mean(dim=axis, keepdim=True)
    v = x.var(dim=axis, keepdim=True, unbiased=False)
    return (x - m) / torch.sqrt(v + eps)
def _scatter_add(tgt, idx, src, axis=0):
    return tgt.clone().index_add_(axis, idx, src)
def _segment_sum(src, seg, n):
    out = torch.zeros((n,) + src.shape[1:], dtype=src.dtype)
    return out.index_add_(0, seg, src)
def _pad(x, pw):
    flat = tuple(v for pair in reversed(pw) for v in pair)
    return torch.nn.functional.pad(x, flat)

kernel_name = 'standalone__gqa_block_cached__near_ridge'

def setup_inputs():

    _w = (lambda a: (lambda t: t.to(torch.bfloat16) if t.is_floating_point() else t)(torch.as_tensor(a)))
    B, Sq, D = 8, 96, 512
    Hq, Hkv, Sk, Dh = 32, 32, 512, 128
    assert Hq % Hkv == 0, (Hq, Hkv)
    return {
        "x": _w((lambda *s: torch.randn(*s, dtype=torch.bfloat16) * 0.8)(B, Sq, D)),
        "k_cache": _w((lambda *s: torch.randn(*s, dtype=torch.bfloat16) * 0.8)(B, Hkv, Sk, Dh)),
        "v_cache": _w((lambda *s: torch.randn(*s, dtype=torch.bfloat16) * 0.05)(B, Hkv, Sk, Dh)),
        "wq": _w((lambda *s: torch.randn(*s, dtype=torch.bfloat16) * 0.8)(D, Hq * Dh) * (D ** -0.5)),
        "wo": _w((lambda *s: torch.randn(*s, dtype=torch.bfloat16) * 0.05)(Hq * Dh, D) * ((Hq * Dh) ** -0.5)),
    }

def reference(x, k_cache, v_cache, wq, wo):
    B, Sq, D = x.shape
    Hq, Dh = 32, 128
    Hkv = k_cache.shape[1]
    rep = Hq // Hkv
    q = (lambda x, *d: x.permute(*d))((x @ wq).reshape(B, Sq, Hq, Dh), 0, 2, 1, 3)
    k = (lambda x, n, axis: torch.repeat_interleave(x, n, dim=axis))(k_cache, rep, axis=1)
    v = (lambda x, n, axis: torch.repeat_interleave(x, n, dim=axis))(v_cache, rep, axis=1)
    scores = torch.einsum("bhqd,bhkd->bhqk", q, k) * (Dh ** -0.5)
    p = _softmax(scores, axis=-1)
    o = torch.einsum("bhqk,bhkd->bhqd", p, v)
    return (lambda x, *d: x.permute(*d))(o, 0, 2, 1, 3).reshape(B, Sq, Hq * Dh) @ wo
```

```cpp
#include <hip/hip_runtime.h>
#include <hip/hip_bf16.h>
#include <utility>

#define LAS __attribute__((address_space(3)))
#define GAS __attribute__((address_space(1)))
#define DI __device__ __forceinline__
typedef __bf16 bf16x8 __attribute__((ext_vector_type(8)));
typedef __bf16 bf16x4 __attribute__((ext_vector_type(4)));
typedef __bf16 bf16x2 __attribute__((ext_vector_type(2)));
typedef float f32x16 __attribute__((ext_vector_type(16)));
typedef float f32x4 __attribute__((ext_vector_type(4)));
typedef float f32x2 __attribute__((ext_vector_type(2)));
typedef unsigned u32x4 __attribute__((ext_vector_type(4)));
typedef unsigned u32x2 __attribute__((ext_vector_type(2)));
typedef unsigned short u16;

constexpr int NB = 8, SQ = 96, DM = 512, NH = 32, SK = 512, DH = 128;
constexpr int NSLOT = 10, SLOTB = 16384, NTILE = 24;

template <int N> DI void wait_vm() { asm volatile("s_waitcnt vmcnt(%0)" ::"n"(N) : "memory"); }
DI void bar() { asm volatile("s_waitcnt lgkmcnt(0)\n\ts_barrier" ::: "memory"); }
DI unsigned cvtpk(float lo, float hi) { f32x2 v = {lo, hi}; bf16x2 b = __builtin_convertvector(v, bf16x2); return __builtin_bit_cast(unsigned, b); }
DI bf16x8 pack8(const f32x16& x, int s) {
  u32x4 w = {cvtpk(x[8 * s], x[8 * s + 1]), cvtpk(x[8 * s + 2], x[8 * s + 3]), cvtpk(x[8 * s + 4], x[8 * s + 5]), cvtpk(x[8 * s + 6], x[8 * s + 7])};
  return __builtin_bit_cast(bf16x8, w);
}
DI bf16x4 trread(unsigned a) { return __builtin_amdgcn_ds_read_tr16_b64_v4bf16((LAS bf16x4*)(size_t)a); }
DI bf16x8 tr2(unsigned a0, unsigned a1) { bf16x4 lo = trread(a0), hi = trread(a1); return __builtin_shufflevector(lo, hi, 0, 1, 2, 3, 4, 5, 6, 7); }
DI float swap32max(float v) { auto r = __builtin_amdgcn_permlane32_swap(__float_as_uint(v), __float_as_uint(v), false, false); return fmaxf(__uint_as_float(r[0]), __uint_as_float(r[1])); }
DI float swap32sum(float v) { auto r = __builtin_amdgcn_permlane32_swap(__float_as_uint(v), __float_as_uint(v), false, false); return __uint_as_float(r[0]) + __uint_as_float(r[1]); }
#define MFMA32(a, b, c) __builtin_amdgcn_mfma_f32_32x32x16_bf16((a), (b), (c), 0, 0, 0)
#define MFMA16(a, b, c) __builtin_amdgcn_mfma_f32_16x16x32_bf16((a), (b), (c), 0, 0, 0)

template <int I, int N, class F> DI void static_for(F&& f) { if constexpr (I < N) { f(std::integral_constant<int, I>{}); static_for<I + 1, N>(f); } }

constexpr int tile_kind(int i) { return i < 8 ? 0 : (i == 8 ? 1 : (i == 23 ? 2 : ((i & 1) ? 1 : 2))); }
constexpr int tile_idx(int i) { return i < 8 ? i : (i == 8 ? 0 : (i == 23 ? 7 : ((i & 1) ? (i - 9) / 2 + 1 : (i - 10) / 2))); }

__global__ __launch_bounds__(512, 2) void attn_kernel(const u16* __restrict__ x, const u16* __restrict__ kc, const u16* __restrict__ vc,
                                                     const u16* __restrict__ wq, u16* __restrict__ ows) {
  extern __shared__ __attribute__((aligned(16))) char smem[];
  const int tid = threadIdx.x, lane = tid & 63;
  const int wid = __builtin_amdgcn_readfirstlane(tid >> 6);
  const int bid = blockIdx.x, xcd = bid & 7, idx = bid >> 3;
  const int h = xcd * 4 + (idx & 3), b = idx >> 2;
  const unsigned sbase = (unsigned)(size_t)(LAS char*)smem;
  if (wid >= 3) {
    if (wid == 7) return;
    const int j = wid - 3;
    const int sub = lane >> 5, r7 = (lane >> 2) & 7, sl = lane & 3;
    const int c0 = (4 * sub + (sl ^ (r7 >> 2))) * 16, c1 = (4 * sub + (sl ^ (2 + (r7 >> 2)))) * 16;
    const unsigned kvo0 = (16 * j + r7) * 256 + c0, kvo1 = (16 * j + 8 + r7) * 256 + c1;
    const unsigned wo0 = (16 * j + r7) * 8192 + c0, wo1 = (16 * j + 8 + r7) * 8192 + c1;
    const char* kb = (const char*)kc + (size_t)(b * NH + h) * SK * DH * 2;
    const char* vb = (const char*)vc + (size_t)(b * NH + h) * SK * DH * 2;
    const char* wb = (const char*)wq + h * DH * 2;
    auto issue = [&](auto I) {
      constexpr int i = I.value, kind = tile_kind(i), ti = tile_idx(i), slot = i % NSLOT;
      const char* g = kind == 0 ? wb + (size_t)ti * 64 * 8192 : (kind == 1 ? kb : vb) + ti * SLOTB;
      const unsigned o0 = kind == 0 ? wo0 : kvo0, o1 = kind == 0 ? wo1 : kvo1;
      LAS char* d = (LAS char*)smem + slot * SLOTB + j * 4096;
      __builtin_amdgcn_global_load_lds((const GAS void*)(g + o0), (LAS void*)(d), 16, 0, 0);
      __builtin_amdgcn_global_load_lds((const GAS void*)(g + o0 + 128), (LAS void*)(d + 1024), 16, 0, 0);
      __builtin_amdgcn_global_load_lds((const GAS void*)(g + o1), (LAS void*)(d + 2048), 16, 0, 0);
      __builtin_amdgcn_global_load_lds((const GAS void*)(g + o1 + 128), (LAS void*)(d + 3072), 16, 0, 0);
    };
    static_for<0, NSLOT>(issue);
    static_for<0, NTILE>([&](auto I) {
      constexpr int i = I.value;
      constexpr int last = (i + 8 < 9 ? 9 : (i + 8 > NTILE - 1 ? NTILE - 1 : i + 8));
      wait_vm<4 * (last - i)>();
      bar();
      if constexpr (i >= 1 && i + 9 < NTILE) issue(std::integral_constant<int, i + 9>{});
    });
    return;
  }
  const int r = lane & 31, hh = lane >> 5, blk = (lane >> 4) & 1, qq = (lane & 15) >> 2, p = lane & 3;
  const int ps = ((p & 1) << 1) | (p >> 1);
  const unsigned wa0 = sbase + 2048 * hh + 64 * qq + 16 * ((2 * blk + (ps >> 1)) ^ (2 * hh)) + 8 * (ps & 1);
  const unsigned wa1 = sbase + 2048 * hh + 64 * (4 + qq) + 16 * ((2 * blk + (ps >> 1)) ^ (2 * hh + 1)) + 8 * (ps & 1);
  const unsigned va0 = sbase + 64 * (4 * hh + qq) + 16 * ((2 * blk + (p >> 1)) ^ hh) + 8 * (p & 1);
  const unsigned va1 = sbase + 2048 + 64 * (4 * hh + qq) + 16 * ((2 * blk + (p >> 1)) ^ (2 + hh)) + 8 * (p & 1);
  const unsigned ka0 = sbase + 2048 * (r >> 3) + 64 * (r & 7) + 16 * (hh ^ ((r >> 2) & 3));
  const unsigned ka1 = sbase + 2048 * (r >> 3) + 64 * (r & 7) + 16 * ((2 + hh) ^ ((r >> 2) & 3));

  const u16* xr = x + (size_t)(b * SQ + wid * 32 + r) * DM + hh * 8;
  bf16x8 xf[16];
#pragma unroll
  for (int s = 0; s < 16; ++s) xf[s] = *(const bf16x8*)(xr + s * 16);
  f32x16 acc[4];
#pragma unroll
  for (int c = 0; c < 4; ++c) acc[c] = f32x16{};
  static_for<0, 8>([&](auto I) {
    constexpr int i = I.value, so = (i % NSLOT) * SLOTB;
    bar();
#pragma unroll
    for (int ks = 0; ks < 4; ++ks) {
#pragma unroll
      for (int c = 0; c < 4; ++c) {
        bf16x8 a = tr2(wa0 + so + 4096 * ks + 512 * c, wa1 + so + 4096 * ks + 512 * c);
        acc[c] = MFMA32(a, xf[(4 * i + ks) & 15], acc[c]);
      }
    }
    if constexpr (i < 4) {
#pragma unroll
      for (int ks = 0; ks < 4; ++ks) xf[4 * i + ks] = *(const bf16x8*)(xr + (16 + 4 * i + ks) * 16);
    }
  });
  bf16x8 qf[8];
#pragma unroll
  for (int c = 0; c < 4; ++c) { qf[2 * c] = pack8(acc[c], 0); qf[2 * c + 1] = pack8(acc[c], 1); }
#pragma unroll
  for (int c = 0; c < 4; ++c) acc[c] = f32x16{};
  const float CS = 0.08838834764831845f * 1.4426950408889634f;
  float m = -__builtin_inff(), l = 0.f, alpha = 1.f;
  bf16x8 pf[2][4];
  static_for<8, NTILE>([&](auto I) {
    constexpr int i = I.value, so = (i % NSLOT) * SLOTB, kind = tile_kind(i), ti = tile_idx(i);
    bar();
    if constexpr (kind == 1) {
      f32x16 s0 = f32x16{}, s1 = f32x16{};
#pragma unroll
      for (int s = 0; s < 8; ++s) {
        const unsigned a = ((s & 1) ? ka1 : ka0) + so + 512 * (s >> 1);
        bf16x8 k0 = *(LAS bf16x8*)(size_t)(a), k1 = *(LAS bf16x8*)(size_t)(a + 8192);
        s0 = MFMA32(k0, qf[s], s0);
        s1 = MFMA32(k1, qf[s], s1);
      }
      float mx = s0[0];
#pragma unroll
      for (int e = 1; e < 16; ++e) mx = fmaxf(mx, s0[e]);
#pragma unroll
      for (int e = 0; e < 16; ++e) mx = fmaxf(mx, s1[e]);
      mx = swap32max(mx);
      const float mn = fmaxf(m, mx);
      alpha = __builtin_amdgcn_exp2f((m - mn) * CS);
      m = mn;
      const float nm = -mn * CS;
      float rs = 0.f;
#pragma unroll
      for (int e = 0; e < 16; ++e) { s0[e] = __builtin_amdgcn_exp2f(fmaf(s0[e], CS, nm)); rs += s0[e]; }
#pragma unroll
      for (int e = 0; e < 16; ++e) { s1[e] = __builtin_amdgcn_exp2f(fmaf(s1[e], CS, nm)); rs += s1[e]; }
      l = l * alpha + rs;
      pf[ti & 1][0] = pack8(s0, 0); pf[ti & 1][1] = pack8(s0, 1); pf[ti & 1][2] = pack8(s1, 0); pf[ti & 1][3] = pack8(s1, 1);
    } else {
#pragma unroll
      for (int ks = 0; ks < 4; ++ks) {
#pragma unroll
        for (int c = 0; c < 4; ++c) {
          bf16x8 a = tr2(va0 + so + 4096 * ks + 512 * c, va1 + so + 4096 * ks + 512 * c);
          acc[c] = MFMA32(a, pf[ti & 1][ks], acc[c]);
        }
      }
      if constexpr (ti < 7) {
#pragma unroll
        for (int c = 0; c < 4; ++c)
#pragma unroll
          for (int e = 0; e < 16; ++e) acc[c][e] *= alpha;
      }
    }
  });
  const float linv = 1.f / swap32sum(l);
  const int u = 2 * wid + (r >> 4);
  char* ob = (char*)ows + ((size_t)((b * 6 + u) * 128 + h * 4) * 64 + hh * 16 + (r & 15)) * 16;
#pragma unroll
  for (int c = 0; c < 4; ++c) {
#pragma unroll
    for (int e = 0; e < 16; ++e) acc[c][e] *= linv;
    *(bf16x8*)(ob + c * 1024) = pack8(acc[c], 0);
    *(bf16x8*)(ob + c * 1024 + 512) = pack8(acc[c], 1);
  }
}

__global__ __launch_bounds__(512, 2) void oproj_kernel(const u16* __restrict__ ows, const u16* __restrict__ wo, u16* __restrict__ out) {
  extern __shared__ __attribute__((aligned(16))) char smem[];
  const int tid = threadIdx.x, lane = tid & 63;
  const int wid = __builtin_amdgcn_readfirstlane(tid >> 6);
  const int bid = blockIdx.x, xcd = bid & 7, idx = bid >> 3;
  const int rb = 4 * (xcd >> 1) + (idx >> 3), cb = 8 * (xcd & 1) + (idx & 7);
  const unsigned sbase = (unsigned)(size_t)(LAS char*)smem + wid * 16384;
  const int row = lane >> 2, ch = (lane & 3) ^ (((row >> 2) & 1) << 1);
  const char* wsrc = (const char*)wo + (size_t)(wid * 512 + row) * 1024 + cb * 64 + ch * 16;
  const char* osrc = (const char*)ows + ((size_t)(rb * 3) * 128 + wid * 16) * 1024 + lane * 16;
  const int g = lane >> 4, qq = (lane & 15) >> 2, p = lane & 3;
  const unsigned ta = sbase + 64 * (16 * (g >> 1) + 4 * (g & 1) + qq) + 16 * ((p >> 1) ^ ((g & 1) << 1)) + 8 * (p & 1);
  f32x4 acc[2][3];
#pragma unroll
  for (int nb = 0; nb < 2; ++nb)
#pragma unroll
    for (int u = 0; u < 3; ++u) acc[nb][u] = f32x4{};
#pragma unroll
  for (int half = 0; half < 2; ++half) {
    if (half) { asm volatile("s_waitcnt lgkmcnt(0)" ::: "memory"); }
#pragma unroll
    for (int pc = 0; pc < 16; ++pc)
      __builtin_amdgcn_global_load_lds((const GAS void*)(wsrc + (size_t)(half * 256 + pc * 16) * 1024), (LAS void*)((LAS char*)smem + wid * 16384 + pc * 1024), 16, 0, 0);
    bf16x8 of[3][8];
#pragma unroll
    for (int u = 0; u < 3; ++u)
#pragma unroll
      for (int k = 0; k < 8; ++k) of[u][k] = *(const bf16x8*)(osrc + ((size_t)u * 128 + half * 8 + k) * 1024);
    asm volatile("s_waitcnt vmcnt(0)" ::: "memory");
#pragma unroll
    for (int k = 0; k < 8; ++k) {
#pragma unroll
      for (int nb = 0; nb < 2; ++nb) {
        const unsigned a = (ta ^ (nb * 32)) + k * 2048;
        bf16x8 af = tr2(a, a + 512);
#pragma unroll
        for (int u = 0; u < 3; ++u) acc[nb][u] = MFMA16(af, of[u][k], acc[nb][u]);
      }
    }
  }
  __syncthreads();
  LAS f32x4* red = (LAS f32x4*)smem;
#pragma unroll
  for (int nb = 0; nb < 2; ++nb)
#pragma unroll
    for (int u = 0; u < 3; ++u) red[(wid * 6 + nb * 3 + u) * 64 + lane] = acc[nb][u];
  __syncthreads();
  if (wid < 6) {
    f32x4 s = red[wid * 64 + lane];
#pragma unroll
    for (int w = 1; w < 8; ++w) s += red[(w * 6 + wid) * 64 + lane];
    const int nb = wid / 3, u = wid % 3;
    const int qrow = rb * 48 + u * 16 + (lane & 15), n = cb * 32 + nb * 16 + g * 4;
    u32x2 o = {cvtpk(s[0], s[1]), cvtpk(s[2], s[3])};
    *(u32x2*)(out + (size_t)qrow * DM + n) = o;
  }
}

extern "C" void kernel_launch(void* const* d_in, const int* in_sizes, int n_in, void* d_out, int out_size, void* d_ws, size_t ws_size,
                              hipStream_t stream) {
  const u16* x = (const u16*)d_in[0];
  const u16* kc = (const u16*)d_in[1];
  const u16* vc = (const u16*)d_in[2];
  const u16* wq = (const u16*)d_in[3];
  const u16* wo = (const u16*)d_in[4];
  u16* ows = (u16*)d_ws;
  (void)hipFuncSetAttribute((const void*)attn_kernel, hipFuncAttributeMaxDynamicSharedMemorySize, NSLOT * SLOTB);
  (void)hipFuncSetAttribute((const void*)oproj_kernel, hipFuncAttributeMaxDynamicSharedMemorySize, 131072);
  attn_kernel<<<256, 512, NSLOT * SLOTB, stream>>>(x, kc, vc, wq, ows);
  oproj_kernel<<<256, 512, 131072, stream>>>(ows, wo, (u16*)d_out);
}
```

```cpp
#include <hip/hip_runtime.h>
#include <hip/hip_bf16.h>
#include <utility>

#define LAS __attribute__((address_space(3)))
#define GAS __attribute__((address_space(1)))
#define DI __device__ __forceinline__
typedef __bf16 bf16x8 __attribute__((ext_vector_type(8)));
typedef __bf16 bf16x4 __attribute__((ext_vector_type(4)));
typedef __bf16 bf16x2 __attribute__((ext_vector_type(2)));
typedef float f32x16 __attribute__((ext_vector_type(16)));
typedef float f32x4 __attribute__((ext_vector_type(4)));
typedef float f32x2 __attribute__((ext_vector_type(2)));
typedef unsigned u32x4 __attribute__((ext_vector_type(4)));
typedef unsigned u32x2 __attribute__((ext_vector_type(2)));
typedef unsigned short u16;

constexpr int NB = 8, SQ = 96, DM = 512, NH = 32, SK = 512, DH = 128;
constexpr int NSLOT = 10, SLOTB = 16384, NTILE = 24;

template <int N> DI void wait_vm() { asm volatile("s_waitcnt vmcnt(%0)" ::"n"(N) : "memory"); }
DI void bar() { asm volatile("s_waitcnt lgkmcnt(0)\n\ts_barrier" ::: "memory"); }
DI unsigned cvtpk(float lo, float hi) { f32x2 v = {lo, hi}; bf16x2 b = __builtin_convertvector(v, bf16x2); return __builtin_bit_cast(unsigned, b); }
DI bf16x8 pack8(const f32x16& x, int s) {
  u32x4 w = {cvtpk(x[8 * s], x[8 * s + 1]), cvtpk(x[8 * s + 2], x[8 * s + 3]), cvtpk(x[8 * s + 4], x[8 * s + 5]), cvtpk(x[8 * s + 6], x[8 * s + 7])};
  return __builtin_bit_cast(bf16x8, w);
}
DI bf16x4 trread(unsigned a) { return __builtin_amdgcn_ds_read_tr16_b64_v4bf16((LAS bf16x4*)(size_t)a); }
DI bf16x8 tr2(unsigned a0, unsigned a1) { bf16x4 lo = trread(a0), hi = trread(a1); return __builtin_shufflevector(lo, hi, 0, 1, 2, 3, 4, 5, 6, 7); }
DI float swap32max(float v) { auto r = __builtin_amdgcn_permlane32_swap(__float_as_uint(v), __float_as_uint(v), false, false); return fmaxf(__uint_as_float(r[0]), __uint_as_float(r[1])); }
DI float swap32sum(float v) { auto r = __builtin_amdgcn_permlane32_swap(__float_as_uint(v), __float_as_uint(v), false, false); return __uint_as_float(r[0]) + __uint_as_float(r[1]); }
#define MFMA32(a, b, c) __builtin_amdgcn_mfma_f32_32x32x16_bf16((a), (b), (c), 0, 0, 0)
#define MFMA16(a, b, c) __builtin_amdgcn_mfma_f32_16x16x32_bf16((a), (b), (c), 0, 0, 0)

template <int I, int N, class F> DI void static_for(F&& f) { if constexpr (I < N) { f(std::integral_constant<int, I>{}); static_for<I + 1, N>(f); } }

constexpr int tile_kind(int i) { return i < 8 ? 0 : (i == 8 ? 1 : (i == 23 ? 2 : ((i & 1) ? 1 : 2))); }
constexpr int tile_idx(int i) { return i < 8 ? i : (i == 8 ? 0 : (i == 23 ? 7 : ((i & 1) ? (i - 9) / 2 + 1 : (i - 10) / 2))); }

__global__ __launch_bounds__(512, 2) void attn_kernel(const u16* __restrict__ x, const u16* __restrict__ kc, const u16* __restrict__ vc,
                                                     const u16* __restrict__ wq, u16* __restrict__ ows) {
  extern __shared__ __attribute__((aligned(16))) char smem[];
  const int tid = threadIdx.x, lane = tid & 63;
  const int wid = __builtin_amdgcn_readfirstlane(tid >> 6);
  const int bid = blockIdx.x, xcd = bid & 7, idx = bid >> 3;
  const int h = xcd * 4 + (idx & 3), b = idx >> 2;
  const unsigned sbase = (unsigned)(size_t)(LAS char*)smem;
  if (wid >= 3) {
    if (wid == 7) return;
    const int j = wid - 3;
    const int sub = lane >> 5, r7 = (lane >> 2) & 7, sl = lane & 3;
    const int c0 = (4 * sub + (sl ^ (r7 >> 2))) * 16, c1 = (4 * sub + (sl ^ (2 + (r7 >> 2)))) * 16;
    const unsigned kvo0 = (16 * j + r7) * 256 + c0, kvo1 = (16 * j + 8 + r7) * 256 + c1;
    const unsigned wo0 = (16 * j + r7) * 8192 + c0, wo1 = (16 * j + 8 + r7) * 8192 + c1;
    const char* kb = (const char*)kc + (size_t)(b * NH + h) * SK * DH * 2;
    const char* vb = (const char*)vc + (size_t)(b * NH + h) * SK * DH * 2;
    const char* wb = (const char*)wq + h * DH * 2;
    auto issue = [&](auto I) {
      constexpr int i = I.value, kind = tile_kind(i), ti = tile_idx(i), slot = i % NSLOT, AUX = 2;
      const char* g = kind == 0 ? wb + (size_t)ti * 64 * 8192 : (kind == 1 ? kb : vb) + ti * SLOTB;
      const unsigned o0 = kind == 0 ? wo0 : kvo0, o1 = kind == 0 ? wo1 : kvo1;
      LAS char* d = (LAS char*)smem + slot * SLOTB + j * 4096;
      __builtin_amdgcn_global_load_lds((const GAS void*)(g + o0), (LAS void*)(d), 16, 0, AUX);
      __builtin_amdgcn_global_load_lds((const GAS void*)(g + o0 + 128), (LAS void*)(d + 1024), 16, 0, AUX);
      __builtin_amdgcn_global_load_lds((const GAS void*)(g + o1), (LAS void*)(d + 2048), 16, 0, AUX);
      __builtin_amdgcn_global_load_lds((const GAS void*)(g + o1 + 128), (LAS void*)(d + 3072), 16, 0, AUX);
    };
    static_for<0, NSLOT>(issue);
    static_for<0, NTILE>([&](auto I) {
      constexpr int i = I.value;
      constexpr int last = (i + 8 < 9 ? 9 : (i + 8 > NTILE - 1 ? NTILE - 1 : i + 8));
      wait_vm<4 * (last - i)>();
      bar();
      if constexpr (i >= 1 && i + 9 < NTILE) issue(std::integral_constant<int, i + 9>{});
    });
    return;
  }
  const int r = lane & 31, hh = lane >> 5, blk = (lane >> 4) & 1, qq = (lane & 15) >> 2, p = lane & 3;
  const int ps = ((p & 1) << 1) | (p >> 1);
  const unsigned wa0 = sbase + 2048 * hh + 64 * qq + 16 * ((2 * blk + (ps >> 1)) ^ (2 * hh)) + 8 * (ps & 1);
  const unsigned wa1 = sbase + 2048 * hh + 64 * (4 + qq) + 16 * ((2 * blk + (ps >> 1)) ^ (2 * hh + 1)) + 8 * (ps & 1);
  const unsigned va0 = sbase + 64 * (4 * hh + qq) + 16 * ((2 * blk + (p >> 1)) ^ hh) + 8 * (p & 1);
  const unsigned va1 = sbase + 2048 + 64 * (4 * hh + qq) + 16 * ((2 * blk + (p >> 1)) ^ (2 + hh)) + 8 * (p & 1);
  const unsigned ka0 = sbase + 2048 * (r >> 3) + 64 * (r & 7) + 16 * (hh ^ ((r >> 2) & 3));
  const unsigned ka1 = sbase + 2048 * (r >> 3) + 64 * (r & 7) + 16 * ((2 + hh) ^ ((r >> 2) & 3));

  const u16* xr = x + (size_t)(b * SQ + wid * 32 + r) * DM + hh * 8;
  bf16x8 xf[16];
#pragma unroll
  for (int s = 0; s < 16; ++s) xf[s] = *(const bf16x8*)(xr + s * 16);
  f32x16 acc[4];
#pragma unroll
  for (int c = 0; c < 4; ++c) acc[c] = f32x16{};
  static_for<0, 8>([&](auto I) {
    constexpr int i = I.value, so = (i % NSLOT) * SLOTB;
    bar();
#pragma unroll
    for (int ks = 0; ks < 4; ++ks) {
#pragma unroll
      for (int c = 0; c < 4; ++c) {
        bf16x8 a = tr2(wa0 + so + 4096 * ks + 512 * c, wa1 + so + 4096 * ks + 512 * c);
        acc[c] = MFMA32(a, xf[(4 * i + ks) & 15], acc[c]);
      }
    }
    if constexpr (i < 4) {
#pragma unroll
      for (int ks = 0; ks < 4; ++ks) xf[4 * i + ks] = *(const bf16x8*)(xr + (16 + 4 * i + ks) * 16);
    }
  });
  bf16x8 qf[8];
#pragma unroll
  for (int c = 0; c < 4; ++c) { qf[2 * c] = pack8(acc[c], 0); qf[2 * c + 1] = pack8(acc[c], 1); }
#pragma unroll
  for (int c = 0; c < 4; ++c) acc[c] = f32x16{};
  const float CS = 0.08838834764831845f * 1.4426950408889634f;
  float m = -__builtin_inff(), l = 0.f, alpha = 1.f;
  bf16x8 pf[2][4];
  static_for<8, NTILE>([&](auto I) {
    constexpr int i = I.value, so = (i % NSLOT) * SLOTB, kind = tile_kind(i), ti = tile_idx(i);
    bar();
    if constexpr (kind == 1) {
      f32x16 s0 = f32x16{}, s1 = f32x16{};
#pragma unroll
      for (int s = 0; s < 8; ++s) {
        const unsigned a = ((s & 1) ? ka1 : ka0) + so + 512 * (s >> 1);
        bf16x8 k0 = *(LAS bf16x8*)(size_t)(a), k1 = *(LAS bf16x8*)(size_t)(a + 8192);
        s0 = MFMA32(k0, qf[s], s0);
        s1 = MFMA32(k1, qf[s], s1);
      }
      float mx = s0[0];
#pragma unroll
      for (int e = 1; e < 16; ++e) mx = fmaxf(mx, s0[e]);
#pragma unroll
      for (int e = 0; e < 16; ++e) mx = fmaxf(mx, s1[e]);
      mx = swap32max(mx);
      const float mn = fmaxf(m, mx);
      alpha = __builtin_amdgcn_exp2f((m - mn) * CS);
      m = mn;
      const float nm = -mn * CS;
      float rs = 0.f;
#pragma unroll
      for (int e = 0; e < 16; ++e) { s0[e] = __builtin_amdgcn_exp2f(fmaf(s0[e], CS, nm)); rs += s0[e]; }
#pragma unroll
      for (int e = 0; e < 16; ++e) { s1[e] = __builtin_amdgcn_exp2f(fmaf(s1[e], CS, nm)); rs += s1[e]; }
      l = l * alpha + rs;
      pf[ti & 1][0] = pack8(s0, 0); pf[ti & 1][1] = pack8(s0, 1); pf[ti & 1][2] = pack8(s1, 0); pf[ti & 1][3] = pack8(s1, 1);
    } else {
#pragma unroll
      for (int ks = 0; ks < 4; ++ks) {
#pragma unroll
        for (int c = 0; c < 4; ++c) {
          bf16x8 a = tr2(va0 + so + 4096 * ks + 512 * c, va1 + so + 4096 * ks + 512 * c);
          acc[c] = MFMA32(a, pf[ti & 1][ks], acc[c]);
        }
      }
      if constexpr (ti < 7) {
#pragma unroll
        for (int c = 0; c < 4; ++c)
#pragma unroll
          for (int e = 0; e < 16; ++e) acc[c][e] *= alpha;
      }
    }
  });
  const float linv = 1.f / swap32sum(l);
  const int u = 2 * wid + (r >> 4);
  char* ob = (char*)ows + ((size_t)((b * 6 + u) * 128 + h * 4) * 64 + hh * 16 + (r & 15)) * 16;
#pragma unroll
  for (int c = 0; c < 4; ++c) {
#pragma unroll
    for (int e = 0; e < 16; ++e) acc[c][e] *= linv;
    *(bf16x8*)(ob + c * 1024) = pack8(acc[c], 0);
    *(bf16x8*)(ob + c * 1024 + 512) = pack8(acc[c], 1);
  }
}

__global__ __launch_bounds__(512, 2) void oproj_kernel(const u16* __restrict__ ows, const u16* __restrict__ wo, u16* __restrict__ out) {
  extern __shared__ __attribute__((aligned(16))) char smem[];
  const int tid = threadIdx.x, lane = tid & 63;
  const int wid = __builtin_amdgcn_readfirstlane(tid >> 6);
  const int bid = blockIdx.x, xcd = bid & 7, idx = bid >> 3;
  const int rb = 4 * (xcd >> 1) + (idx >> 3), cb = 8 * (xcd & 1) + (idx & 7);
  const unsigned sbase = (unsigned)(size_t)(LAS char*)smem + wid * 16384;
  const int row = lane >> 2, ch = (lane & 3) ^ (((row >> 2) & 1) << 1);
  const char* wsrc = (const char*)wo + (size_t)(wid * 512 + row) * 1024 + cb * 64 + ch * 16;
  const char* osrc = (const char*)ows + ((size_t)(rb * 3) * 128 + wid * 16) * 1024 + lane * 16;
  const int g = lane >> 4, qq = (lane & 15) >> 2, p = lane & 3;
  const unsigned ta = sbase + 64 * (16 * (g >> 1) + 4 * (g & 1) + qq) + 16 * ((p >> 1) ^ ((g & 1) << 1)) + 8 * (p & 1);
  f32x4 acc[2][3];
#pragma unroll
  for (int nb = 0; nb < 2; ++nb)
#pragma unroll
    for (int u = 0; u < 3; ++u) acc[nb][u] = f32x4{};
#pragma unroll
  for (int half = 0; half < 2; ++half) {
    if (half) { asm volatile("s_waitcnt lgkmcnt(0)" ::: "memory"); }
#pragma unroll
    for (int pc = 0; pc < 16; ++pc)
      __builtin_amdgcn_global_load_lds((const GAS void*)(wsrc + (size_t)(half * 256 + pc * 16) * 1024), (LAS void*)((LAS char*)smem + wid * 16384 + pc * 1024), 16, 0, 0);
    bf16x8 of[3][8];
#pragma unroll
    for (int u = 0; u < 3; ++u)
#pragma unroll
      for (int k = 0; k < 8; ++k) of[u][k] = *(const bf16x8*)(osrc + ((size_t)u * 128 + half * 8 + k) * 1024);
    asm volatile("s_waitcnt vmcnt(0)" ::: "memory");
#pragma unroll
    for (int k = 0; k < 8; ++k) {
#pragma unroll
      for (int nb = 0; nb < 2; ++nb) {
        const unsigned a = (ta ^ (nb * 32)) + k * 2048;
        bf16x8 af = tr2(a, a + 512);
#pragma unroll
        for (int u = 0; u < 3; ++u) acc[nb][u] = MFMA16(af, of[u][k], acc[nb][u]);
      }
    }
  }
  __syncthreads();
  LAS f32x4* red = (LAS f32x4*)smem;
#pragma unroll
  for (int nb = 0; nb < 2; ++nb)
#pragma unroll
    for (int u = 0; u < 3; ++u) red[(wid * 6 + nb * 3 + u) * 64 + lane] = acc[nb][u];
  __syncthreads();
  if (wid < 6) {
    f32x4 s = red[wid * 64 + lane];
#pragma unroll
    for (int w = 1; w < 8; ++w) s += red[(w * 6 + wid) * 64 + lane];
    const int nb = wid / 3, u = wid % 3;
    const int qrow = rb * 48 + u * 16 + (lane & 15), n = cb * 32 + nb * 16 + g * 4;
    u32x2 o = {cvtpk(s[0], s[1]), cvtpk(s[2], s[3])};
    *(u32x2*)(out + (size_t)qrow * DM + n) = o;
  }
}

extern "C" void kernel_launch(void* const* d_in, const int* in_sizes, int n_in, void* d_out, int out_size, void* d_ws, size_t ws_size,
                              hipStream_t stream) {
  const u16* x = (const u16*)d_in[0];
  const u16* kc = (const u16*)d_in[1];
  const u16* vc = (const u16*)d_in[2];
  const u16* wq = (const u16*)d_in[3];
  const u16* wo = (const u16*)d_in[4];
  u16* ows = (u16*)d_ws;
  (void)hipFuncSetAttribute((const void*)attn_kernel, hipFuncAttributeMaxDynamicSharedMemorySize, NSLOT * SLOTB);
  (void)hipFuncSetAttribute((const void*)oproj_kernel, hipFuncAttributeMaxDynamicSharedMemorySize, 131072);
  attn_kernel<<<256, 512, NSLOT * SLOTB, stream>>>(x, kc, vc, wq, ows);
  oproj_kernel<<<256, 512, 131072, stream>>>(ows, wo, (u16*)d_out);
}
```

```cpp
#include <hip/hip_runtime.h>
#include <hip/hip_bf16.h>
#include <utility>

#define LAS __attribute__((address_space(3)))
#define GAS __attribute__((address_space(1)))
#define DI __device__ __forceinline__
typedef __bf16 bf16x8 __attribute__((ext_vector_type(8)));
typedef __bf16 bf16x4 __attribute__((ext_vector_type(4)));
typedef __bf16 bf16x2 __attribute__((ext_vector_type(2)));
typedef float f32x16 __attribute__((ext_vector_type(16)));
typedef float f32x4 __attribute__((ext_vector_type(4)));
typedef float f32x2 __attribute__((ext_vector_type(2)));
typedef unsigned u32x4 __attribute__((ext_vector_type(4)));
typedef unsigned u32x2 __attribute__((ext_vector_type(2)));
typedef unsigned short u16;
typedef unsigned long long u64;
typedef GAS u64 gu64;
#define RLX_AGENT __ATOMIC_RELAXED, __HIP_MEMORY_SCOPE_AGENT
#define RLX_WG __ATOMIC_RELAXED, __HIP_MEMORY_SCOPE_WORKGROUP

#define K_NT 1
#define V_NT 1
#define WIN 3
#define DESYNC 1
#define AUX_AT 12

constexpr int NB = 8, SQ = 96, DM = 512, NH = 32, SK = 512, DH = 128;
constexpr int NSLOT = 9, SLOTB = 16384, NT = 24;
constexpr int CTRL = NSLOT * SLOTB;
constexpr int SCR = CTRL + 1024;
constexpr int SCR2 = SCR + 12288;
constexpr int LDS_BYTES = 163840;
constexpr int W_FULL = 0, W_DONE = 4, W_OCNT = 8, W_TAG = 12, W_WT = 15;
constexpr size_t WS_EPOCH = 0, WS_FLAGS = 4096, WS_FLAG2 = 40960, WS_O = 65536, O_BYTES = (size_t)NB * 6 * 128 * 1024, WS_WT = WS_O + O_BYTES, WT_BYTES = (size_t)NH * DH * DM * 2;
constexpr int FLAG_STRIDE = 16;
constexpr unsigned SPIN_CAP = 1u << 20;
constexpr u64 TAG_STEP = 0x9E3779B97F4A7C15ull;

constexpr int slot_tab[NT] = {0, 1, 2, 3, 4, 5, 0, 1, 6, 7, 8, 2, 3, 4, 5, 0, 1, 6, 7, 8, 2, 3, 4, 5};
constexpr u64 pack_slots(int lo) { u64 v = 0; for (int i = 0; i < 16 && lo + i < NT; ++i) v |= (u64)slot_tab[lo + i] << (4 * i); return v; }
constexpr u64 SLOT_LO = pack_slots(0), SLOT_HI = pack_slots(16);
DI int slot_of(int c) { return (int)(((c < 16 ? SLOT_LO : SLOT_HI) >> (4 * (c & 15))) & 15); }

#define SB() __builtin_amdgcn_sched_barrier(0)
#define CFENCE() asm volatile("" ::: "memory")
template <int N> DI void wait_vm() { static_assert(N < 64, "vmcnt"); asm volatile("s_waitcnt vmcnt(%0)" ::"n"(N) : "memory"); }
DI void bar() { asm volatile("s_waitcnt lgkmcnt(0)\n\ts_barrier" ::: "memory"); }
DI unsigned cvtpk(float lo, float hi) { f32x2 v = {lo, hi}; bf16x2 b = __builtin_convertvector(v, bf16x2); return __builtin_bit_cast(unsigned, b); }
DI bf16x8 pack8(const f32x16& x, int s) {
  u32x4 w = {cvtpk(x[8 * s], x[8 * s + 1]), cvtpk(x[8 * s + 2], x[8 * s + 3]), cvtpk(x[8 * s + 4], x[8 * s + 5]), cvtpk(x[8 * s + 6], x[8 * s + 7])};
  return __builtin_bit_cast(bf16x8, w);
}
DI bf16x4 trread(unsigned a) { return __builtin_amdgcn_ds_read_tr16_b64_v4bf16((LAS bf16x4*)(size_t)a); }
DI bf16x8 tr2(unsigned a0, unsigned a1) { bf16x4 lo = trread(a0), hi = trread(a1); return __builtin_shufflevector(lo, hi, 0, 1, 2, 3, 4, 5, 6, 7); }
DI float swap32max(float v) { auto r = __builtin_amdgcn_permlane32_swap(__float_as_uint(v), __float_as_uint(v), false, false); return fmaxf(__uint_as_float(r[0]), __uint_as_float(r[1])); }
DI float swap32sum(float v) { auto r = __builtin_amdgcn_permlane32_swap(__float_as_uint(v), __float_as_uint(v), false, false); return __uint_as_float(r[0]) + __uint_as_float(r[1]); }
DI int lane_id() { int l; asm volatile("v_mbcnt_lo_u32_b32 %0, -1, 0\n\tv_mbcnt_hi_u32_b32 %0, -1, %0" : "=v"(l)); return l; }
DI unsigned opaque(unsigned v) { asm volatile("" : "+v"(v)); return v; }
#define MFMA32(a, b, c) __builtin_amdgcn_mfma_f32_32x32x16_bf16((a), (b), (c), 0, 0, 0)
#define MFMA16(a, b, c) __builtin_amdgcn_mfma_f32_16x16x32_bf16((a), (b), (c), 0, 0, 0)
#define SGB(mask, n) __builtin_amdgcn_sched_group_barrier((mask), (n), 0)
template <int I, int N, class F> DI void static_for(F&& f) { if constexpr (I < N) { f(std::integral_constant<int, I>{}); static_for<I + 1, N>(f); } }
template <int I> using IC = std::integral_constant<int, I>;

template <bool NTL> DI void dma4(unsigned lds, const char* g, unsigned v0, unsigned v1, unsigned v2, unsigned v3) {
  unsigned keep;
  if constexpr (NTL)
    asm volatile("s_mov_b32 %0, m0\n\ts_mov_b32 m0, %1\n\ts_nop 3\n\tglobal_load_lds_dwordx4 %3, %2 nt\n\t"
                 "s_add_u32 m0, m0, 0x400\n\ts_nop 0\n\tglobal_load_lds_dwordx4 %4, %2 nt\n\t"
                 "s_add_u32 m0, m0, 0x400\n\ts_nop 0\n\tglobal_load_lds_dwordx4 %5, %2 nt\n\t"
                 "s_add_u32 m0, m0, 0x400\n\ts_nop 0\n\tglobal_load_lds_dwordx4 %6, %2 nt\n\ts_mov_b32 m0, %0"
                 : "=&s"(keep) : "s"(lds), "s"(g), "v"(v0), "v"(v1), "v"(v2), "v"(v3) : "memory", "scc");
  else
    asm volatile("s_mov_b32 %0, m0\n\ts_mov_b32 m0, %1\n\ts_nop 3\n\tglobal_load_lds_dwordx4 %3, %2\n\t"
                 "s_add_u32 m0, m0, 0x400\n\ts_nop 0\n\tglobal_load_lds_dwordx4 %4, %2\n\t"
                 "s_add_u32 m0, m0, 0x400\n\ts_nop 0\n\tglobal_load_lds_dwordx4 %5, %2\n\t"
                 "s_add_u32 m0, m0, 0x400\n\ts_nop 0\n\tglobal_load_lds_dwordx4 %6, %2\n\ts_mov_b32 m0, %0"
                 : "=&s"(keep) : "s"(lds), "s"(g), "v"(v0), "v"(v1), "v"(v2), "v"(v3) : "memory", "scc");
}

#define STAMP(i) do {} while (0)
#define STAMPL(i) do {} while (0)
#define CSTAMP(i) do {} while (0)

__global__ __launch_bounds__(512, 2) void fused_kernel(const u16* __restrict__ x, const u16* __restrict__ kc, const u16* __restrict__ vc,
                                                      const u16* __restrict__ wq, const u16* __restrict__ wo, u16* __restrict__ out, char* ws) {
  extern __shared__ __attribute__((aligned(16))) char smem[];
  const int tid = threadIdx.x;
  const int wid = __builtin_amdgcn_readfirstlane(tid >> 6);
  const int bid = blockIdx.x, xcd = bid & 7, idx = bid >> 3;
  const int h = xcd * 4 + (idx & 3), b = idx >> 2;
  const int rb = 4 * (xcd >> 1) + (idx >> 3), cb = 8 * (xcd & 1) + (idx & 7);
  const int rot = (b + 2 * (idx & 3)) & 7;
  const int late = DESYNC ? (b & 1) * 3 : 0;
  const unsigned sbase = (unsigned)(size_t)(LAS char*)smem;
  volatile LAS unsigned* ctl = (volatile LAS unsigned*)(size_t)(sbase + CTRL);
  u32x4 wf[32];
  u64 tag;
  const __amdgpu_buffer_rsrc_t ors = __builtin_amdgcn_make_buffer_rsrc(ws + WS_O, 0, (int)(O_BYTES + WT_BYTES), 0x00020000);
  const unsigned wt_off = (unsigned)O_BYTES + (unsigned)(cb * 128 + wid * 16) * 2048;
  unsigned done_seen = 0;
  auto wait_done = [&](unsigned need) {
    if (done_seen < need) {
#pragma unroll 1
      for (unsigned sp = 0; sp < SPIN_CAP; ++sp) {
        const u32x4 d = *(volatile LAS u32x4*)(size_t)(sbase + CTRL + 4 * W_DONE);
        done_seen = __builtin_amdgcn_readfirstlane(min(d.x, min(d.y, d.z)));
        if (done_seen >= need) break;
        __builtin_amdgcn_s_sleep(1);
      }
    }
    CFENCE();
  };
  auto wait_word = [&](int w) {
#pragma unroll 1
    for (unsigned sp = 0; sp < SPIN_CAP; ++sp) { if (__builtin_amdgcn_readfirstlane(ctl[w]) != 0u) break; __builtin_amdgcn_s_sleep(1); }
    CFENCE();
  };
  auto get_tag = [&]() { wait_word(W_TAG + 2); tag = (u64)ctl[W_TAG] | ((u64)ctl[W_TAG + 1] << 32); };

  if (wid >= 4) {
    const int lane = tid & 63, q = wid - 4;
    const int r7 = lane >> 3, sl = lane & 7;
    const unsigned c0 = (sl ^ (((r7 & 2) << 1) | (r7 >> 2))) * 16, c1 = c0 ^ 32;
    const unsigned kv0 = r7 * 256 + c0, kv2 = (8 + r7) * 256 + c1, w0 = r7 * 8192 + c0, w2 = (8 + r7) * 8192 + c1;
    const char* kb = (const char*)kc + (size_t)(b * NH + h) * SK * DH * 2 + q * 4096;
    const char* vb = (const char*)vc + (size_t)(b * NH + h) * SK * DH * 2 + q * 4096;
    const char* wb = (const char*)wq + h * DH * 2 + q * 16 * 8192;
    const unsigned ldsq = sbase + q * 4096;
    auto step = [&](auto N, auto C) {
      constexpr int n = N.value, c = C.value;
      if constexpr (n == WIN) bar();
      if constexpr (c == 6 || c == 7 || c > 10) wait_done(c < 8 ? c - 5 : c - 8);
      const unsigned lds = ldsq + slot_tab[c] * SLOTB;
      if constexpr (c < 8) dma4<false>(lds, wb + (size_t)((c + rot) & 7) * 64 * 8192, w0, w0 + 128, w2, w2 + 128);
      else {
        constexpr bool isk = (c == 8) || ((c & 1) && c != 23);
        constexpr int ti = c == 8 ? 0 : (c == 23 ? 7 : ((c & 1) ? (c - 7) >> 1 : (c - 10) >> 1));
        if constexpr (isk) dma4<K_NT != 0>(lds, kb + ti * SLOTB, kv0, kv0 + 128, kv2, kv2 + 128);
        else dma4<V_NT != 0>(lds, vb + ti * SLOTB, kv0, kv0 + 128, kv2, kv2 + 128);
      }
      if constexpr (n >= WIN) { wait_vm<4 * WIN>(); ctl[W_FULL + q] = (unsigned)(n - WIN + 1); CFENCE(); STAMP(1 + n - WIN); }
    };
    step(IC<0>{}, IC<0>{});
    if (DESYNC != 0 && late) static_for<1, 11>([&](auto N) { step(N, IC<(N.value <= 3 ? N.value + 7 : N.value - 3)>{}); });
    else static_for<1, 11>([&](auto N) { step(N, N); });
    static_for<11, NT>([&](auto N) { step(N, N); });
    wait_word(W_WT);
    static_for<0, WIN>([&](auto I) {
      constexpr int i = I.value;
#pragma unroll
      for (int e = 0; e < 4; ++e) wf[4 * i + e] = __builtin_amdgcn_raw_buffer_load_b128(ors, wt_off + lane * 16, (4 * i + e) * 1024, 16);
      SB();
      wait_vm<4 * WIN>(); ctl[W_FULL + q] = (unsigned)(NT - WIN + i + 1); CFENCE(); STAMP(1 + NT - WIN + i);
    });
#pragma unroll
    for (int e = 4 * WIN; e < 32; ++e) wf[e] = __builtin_amdgcn_raw_buffer_load_b128(ors, wt_off + lane * 16, e * 1024, 16);
    get_tag();
    STAMP(33);
  } else if (wid == 3) {
    const int lane = tid & 63;
    const int ks = bid >> 1, half = bid & 1;
    {
      const char* src = (const char*)wo + (size_t)(ks * 32 + (lane >> 3)) * 1024 + half * 512 + (lane & 7) * 16;
      u32x4 raw[16];
      tag = __hip_atomic_load((gu64*)(ws + WS_EPOCH), RLX_AGENT);
#pragma unroll
      for (int j = 0; j < 4; ++j)
#pragma unroll
        for (int a = 0; a < 4; ++a) raw[4 * j + a] = __builtin_nontemporal_load((const u32x4*)(src + a * 8192 + j * 128));
      SB();
      if (lane < 16) ctl[lane] = 0u;
      bar();
      const int g4 = lane >> 4, qq = (lane & 15) >> 2, p = lane & 3;
      const unsigned sc = sbase + SCR2, ta = sc + 128 * (4 * (g4 & 1) + qq) + 32 * (g4 >> 1) + 8 * p;
      const unsigned dst = (unsigned)O_BYTES + (unsigned)(half * 8 * 128 + ks) * 2048 + (lane >> 5) * 1024 + (lane & 31) * 16;
#pragma unroll
      for (int ps = 0; ps < 8; ++ps) {
        const int j = ps >> 1, hf = ps & 1;
        *(LAS u32x4*)(size_t)(sc + lane * 16) = raw[4 * j + 2 * hf];
        *(LAS u32x4*)(size_t)(sc + 1024 + lane * 16) = raw[4 * j + 2 * hf + 1];
        CFENCE();
#pragma unroll
        for (int m = 0; m < 2; ++m) {
          const bf16x8 f = tr2(ta + m * 64, ta + m * 64 + 1024);
          __builtin_amdgcn_raw_buffer_store_b128(__builtin_bit_cast(u32x4, f), ors, dst + hf * 512, (2 * j + m) * 128 * 2048, 16);
        }
        CFENCE();
      }
      ctl[W_TAG] = (unsigned)tag; ctl[W_TAG + 1] = (unsigned)(tag >> 32);
      CFENCE();
      ctl[W_TAG + 2] = 1u;
      wait_vm<0>();
      if (lane == 0) __hip_atomic_store((gu64*)(ws + WS_FLAG2) + half * 128 + ks, tag + TAG_STEP, RLX_AGENT);
    }
    STAMP(1);
    wait_done(AUX_AT);
    {
      gu64* fp = (gu64*)(ws + WS_FLAG2) + (cb >> 3) * 128 + lane;
#pragma unroll 1
      for (unsigned sp = 0; sp < SPIN_CAP; ++sp) {
        const u64 f0 = __hip_atomic_load(fp, RLX_AGENT), f1 = __hip_atomic_load(fp + 64, RLX_AGENT);
        if (__all(f0 == tag + TAG_STEP && f1 == tag + TAG_STEP)) break;
        __builtin_amdgcn_s_sleep(4);
      }
      CFENCE();
      ctl[W_WT] = 1u;
      CFENCE();
    }
    STAMP(2);
    wait_done(NT - 3);
#pragma unroll
    for (int e = 0; e < 32; ++e) wf[e] = __builtin_amdgcn_raw_buffer_load_b128(ors, wt_off + lane * 16, e * 1024, 16);
    STAMP(33);
  } else {
    int lane = tid & 63;
    int r = lane & 31, hh = lane >> 5, blk = (lane >> 4) & 1, qq = (lane & 15) >> 2, p = lane & 3;
    const int ps = ((p & 1) << 1) | (p >> 1);
    const __amdgpu_buffer_rsrc_t xrs = __builtin_amdgcn_make_buffer_rsrc((void*)(x + (size_t)(b * SQ + wid * 32) * DM), 0, 32 * DM * 2, 0x00020000);
    const unsigned xo = (lane >> 3) * (DM * 2) + (lane & 7) * 16;
    bf16x8 xf[32];
#pragma unroll
    for (int i = 0; i < 8; ++i) {
      const unsigned so = ((i + rot) & 7) * 128;
#pragma unroll
      for (int a = 0; a < 4; ++a) xf[4 * i + a] = __builtin_bit_cast(bf16x8, __builtin_amdgcn_raw_buffer_load_b128(xrs, xo, so + a * 8 * (DM * 2), 0));
      SB();
      if (i == 1) bar();
    }
    const unsigned xsc = sbase + SCR + wid * 4096;
    const unsigned xw = xsc + (lane >> 3) * 128 + 16 * ((lane & 7) ^ (lane >> 4));
    const unsigned xrd = xsc + r * 128 + 16 * (hh ^ ((r >> 1) & 7));
    auto xwr = [&](int i, int a) { *(LAS bf16x8*)(size_t)((xw ^ ((a & 1) * 64)) + a * 1024) = xf[4 * i + a]; CFENCE(); };
    auto xrdf = [&](int i, int ks) { xf[4 * i + ks] = *(LAS bf16x8*)(size_t)(xrd ^ (ks * 32)); CFENCE(); };
    u32x4 fl;
    volatile LAS u32x4* fullp = (volatile LAS u32x4*)(size_t)(sbase + CTRL + 4 * W_FULL);
    auto min4 = [](u32x4 v) { return (unsigned)__builtin_amdgcn_readfirstlane(min(min(v.x, v.y), min(v.z, v.w))); };
    auto peek = [&]() { fl = *fullp; };
    auto wait_full = [&](int n) {
      if (__builtin_expect(min4(fl) <= (unsigned)n, 0)) {
#pragma unroll 1
        for (unsigned sp = 0; sp < SPIN_CAP; ++sp) { if (min4(*fullp) > (unsigned)n) break; }
      }
      CFENCE();
    };
    auto release = [&](int c) { CFENCE(); ctl[W_DONE + wid] = (unsigned)(c + 1); CFENCE(); };
    const unsigned wa = sbase + 2048 * hh + 128 * qq + 16 * ((2 * blk + (ps >> 1)) ^ (((qq & 2) << 1) | (2 * hh))) + 8 * (ps & 1);
    f32x16 acc[4];
#pragma unroll
    for (int c = 0; c < 4; ++c) acc[c] = f32x16{};
    {
      bf16x8 F[3][4];
      unsigned bb[4];
      auto setb = [&](unsigned a, unsigned tx) { bb[0] = opaque(a); bb[1] = opaque(a ^ tx); bb[2] = opaque(a ^ 64); bb[3] = opaque(a ^ (64 ^ tx)); };
      auto wfrag = [&](int ks, int c) { return tr2(bb[2 * (c & 1)] + 4096 * ks + 1024 * (c >> 1), bb[2 * (c & 1) + 1] + 4096 * ks + 1024 * (c >> 1) + 512); };
#pragma unroll
      for (int a = 0; a < 4; ++a) xwr(0, a);
#pragma unroll
      for (int a = 0; a < 4; ++a) xrdf(0, a);
      peek(); wait_full(0); STAMP(1);
      setb(wa + slot_tab[0] * SLOTB, 16);
#pragma unroll
      for (int st = 0; st < 2; ++st)
#pragma unroll
        for (int c = 0; c < 4; ++c) F[st][c] = wfrag(st, c);
      SB();
      static_for<0, 32>([&](auto S) {
        constexpr int st = S.value, nx = st + 2, tile = nx >> 2, ks = nx & 3;
        if constexpr (nx < 32 && ks == 0) { wait_full(tile + late); STAMP(1 + tile); setb(wa + slot_tab[tile] * SLOTB, 16); SB(); }
        static_for<0, 4>([&](auto C) {
          constexpr int c = C.value;
          acc[c] = MFMA32(F[st % 3][c], xf[st], acc[c]);
          SB();
          if constexpr (nx < 32) F[nx % 3][c] = wfrag(ks, c);
          if constexpr ((st & 3) == 0 && st / 4 + 1 < 8) xwr(st / 4 + 1, c);
          if constexpr ((st & 3) == 1 && st / 4 + 1 < 8) xrdf(st / 4 + 1, c);
          if constexpr (c == 3 && nx < 32 && ks == 3) release(tile);
          if constexpr (c == 0 && ks == 3) peek();
          SB();
        });
      });
    }
    bf16x8 qf[8];
#pragma unroll
    for (int c = 0; c < 4; ++c) { qf[2 * c] = pack8(acc[c], 0); qf[2 * c + 1] = pack8(acc[c], 1); }
#pragma unroll
    for (int c = 0; c < 4; ++c) acc[c] = f32x16{};
    lane = lane_id(); r = lane & 31; hh = lane >> 5; blk = (lane >> 4) & 1; qq = (lane & 15) >> 2; p = lane & 3;
    const unsigned va = sbase + 128 * (4 * hh + qq) + 16 * ((2 * blk + (p >> 1)) ^ (((qq & 2) << 1) | hh)) + 8 * (p & 1);
    const unsigned ka = sbase + 2048 * (r >> 3) + 128 * (r & 7) + 16 * (hh ^ (((r & 2) << 1) | ((r >> 2) & 3)));
    const float CS = 0.08838834764831845f * 1.4426950408889634f;
    const float THR = 8.f / CS;
    float m, l = 0.f, alpha = 1.f, nm, rs0, rs1;
    bool pend = false;
    f32x16 sa0, sa1, sb0, sb1;
    u32x4 pf[4];
    auto pin_acc = [&]() { asm volatile("" : "+v"(acc[0]), "+v"(acc[1]), "+v"(acc[2]), "+v"(acc[3])); };
    auto rescale = [&]() {
      if (__builtin_expect(pend, 0)) {
#pragma unroll
        for (int cc = 0; cc < 4; ++cc)
#pragma unroll
          for (int e = 0; e < 16; ++e) acc[cc][e] *= alpha;
        pend = false;
      }
    };
    auto expstage = [&](f32x16& s, int i) {
      if (i + 1 >= 0 && i + 1 < 16) s[i + 1] = fmaf(s[i + 1], CS, nm);
      if (i >= 0 && i < 16) s[i] = __builtin_amdgcn_exp2f(s[i]);
      if (i - 1 >= 0 && i - 1 < 16) { if ((i - 1) & 1) rs1 += s[i - 1]; else rs0 += s[i - 1]; }
    };
    auto packw = [&](f32x16& s0, f32x16& s1, int w) { const f32x16& s = w < 8 ? s0 : s1; pf[w >> 2][w & 3] = cvtpk(s[2 * (w & 7)], s[2 * (w & 7) + 1]); };
    auto qk = [&](int c, f32x16& n0, f32x16& n1, f32x16& o0, f32x16& o1, auto FIN) {
      constexpr bool fin = FIN.value;
      wait_full(c);
      if constexpr (fin) STAMPL(1 + c); else STAMP(9);
      const unsigned a = ka + slot_of(c) * SLOTB;
      const unsigned ab[4] = {opaque(a), opaque(a ^ 32), opaque(a ^ 64), opaque(a ^ 96)};
      bf16x8 kf[16];
      auto ldk = [&](int i) { kf[i] = *(LAS bf16x8*)(size_t)(ab[(i >> 1) & 3] + 1024 * (i >> 3) + 8192 * (i & 1)); };
      constexpr int D = 6;
#pragma unroll
      for (int i = 0; i < D; ++i) ldk(i);
      peek();
      SB();
      if constexpr (fin) {
        rescale();
        expstage(o1, -1); packw(o0, o1, 0); packw(o0, o1, 1); SB();
        expstage(o1, 0); packw(o0, o1, 2); SB();
        expstage(o1, 1); packw(o0, o1, 3); SB();
        expstage(o1, 2); SB();
      }
      static_for<0, 16>([&](auto I) {
        constexpr int i = I.value;
        if constexpr (i & 1) n1 = MFMA32(kf[i], qf[i >> 1], (i < 2 ? f32x16{} : n1)); else n0 = MFMA32(kf[i], qf[i >> 1], (i < 2 ? f32x16{} : n0));
        SB();
        if constexpr (i + D < 16) ldk(i + D);
        if constexpr (i + D == 16) release(c);
        if constexpr (fin) {
          expstage(o1, i + 3);
          if constexpr (i < 4) packw(o0, o1, 4 + i);
          if constexpr (i >= 7 && i < 15) packw(o0, o1, 8 + (i - 7));
          if constexpr (i == 15) l += rs0 + rs1;
        }
        SB();
      });
      asm volatile("" : "+v"(n0), "+v"(n1));
      if constexpr (fin) asm volatile("" : "+v"(pf[0]), "+v"(pf[1]), "+v"(pf[2]), "+v"(pf[3]), "+v"(l));
      SB();
    };
    float mxa, mxb;
    auto maxstage = [&](f32x16& s0, f32x16& s1, int i) {
      if (i == 0) { mxa = fmaxf(s0[0], s0[1]); mxb = fmaxf(s1[0], s1[1]); }
      else { mxa = fmaxf(fmaxf(mxa, s0[2 * i]), s0[2 * i + 1]); mxb = fmaxf(fmaxf(mxb, s1[2 * i]), s1[2 * i + 1]); }
    };
    auto decide = [&](bool first) {
      const float mx = swap32max(fmaxf(mxa, mxb));
      if (first) { m = mx; }
      else {
        const bool grow = __any(mx > m + THR);
        const float mn = grow ? fmaxf(m, mx) : m;
        alpha = __builtin_amdgcn_exp2f((m - mn) * CS);
        l *= alpha;
        m = mn; pend = grow;
      }
      nm = -m * CS; rs0 = 0.f; rs1 = 0.f;
    };
    auto pv = [&](int c, f32x16& n0, f32x16& n1, auto ST) {
      constexpr bool st = ST.value;
      wait_full(c);
      if constexpr (st) STAMPL(1 + c); else STAMP(24);
      const unsigned a = va + slot_of(c) * SLOTB;
      const unsigned ab[4] = {opaque(a), opaque(a ^ 32), opaque(a ^ 64), opaque(a ^ 96)};
      bf16x8 vf[16];
      auto ldv = [&](int i) { const int ks = i >> 2, cc = i & 3; vf[i] = tr2(ab[2 * (cc & 1)] + 4096 * ks + 1024 * (cc >> 1), ab[2 * (cc & 1) + 1] + 4096 * ks + 1024 * (cc >> 1) + 2048); };
      constexpr int D = 5;
#pragma unroll
      for (int i = 0; i < D; ++i) ldv(i);
      peek();
      SB();
      if constexpr (st) {
#pragma unroll
        for (int i = 0; i < 5; ++i) maxstage(n0, n1, i);
        SB();
      }
      static_for<0, 16>([&](auto I) {
        constexpr int i = I.value;
        acc[i & 3] = MFMA32(vf[i], __builtin_bit_cast(bf16x8, pf[i >> 2]), acc[i & 3]);
        SB();
        if constexpr (i + D < 16) ldv(i + D);
        if constexpr (i + D == 16) release(c);
        if constexpr (st) {
          if constexpr (i == 0) { maxstage(n0, n1, 5); maxstage(n0, n1, 6); }
          if constexpr (i == 1) maxstage(n0, n1, 7);
          if constexpr (i == 2) decide(false);
          if constexpr (i >= 3 && i < 11) expstage(n0, i - 4);
          if constexpr (i >= 11) { expstage(n0, 7 + 2 * (i - 11)); SB(); expstage(n0, 8 + 2 * (i - 11)); }
        }
        SB();
      });
      pin_acc();
      if constexpr (st) asm volatile("" : "+v"(n0), "+v"(l), "+v"(alpha), "+v"(nm), "+v"(rs0), "+v"(rs1));
      SB();
    };
    using T = std::true_type; using F_ = std::false_type;
    qk(8, sa0, sa1, sa0, sa1, F_{});
#pragma unroll
    for (int i = 0; i < 8; ++i) maxstage(sa0, sa1, i);
    decide(true);
#pragma unroll
    for (int i = -1; i <= 16; ++i) { expstage(sa0, i); SB(); }
#pragma unroll 1
    for (int t = 0; t < 3; ++t) {
      qk(9 + 4 * t, sb0, sb1, sa0, sa1, T{});
      pv(10 + 4 * t, sb0, sb1, T{});
      qk(11 + 4 * t, sa0, sa1, sb0, sb1, T{});
      pv(12 + 4 * t, sa0, sa1, T{});
    }
    qk(21, sb0, sb1, sa0, sa1, T{});
    pv(22, sb0, sb1, T{});
    rescale();
#pragma unroll
    for (int i = -1; i <= 16; ++i) { expstage(sb1, i); SB(); }
#pragma unroll
    for (int w = 0; w < 16; ++w) packw(sb0, sb1, w);
    l += rs0 + rs1;
    const float linv = 1.f / swap32sum(l);
    pin_acc();
    get_tag();
    pv(23, sb0, sb1, F_{});
    STAMP(25);
    lane = lane_id(); r = lane & 31; hh = lane >> 5;
    const int u = 2 * wid + (r >> 4);
    const unsigned ob = (((b * 6 + u) * 128 + h * 4) * 64 + hh * 16 + (r & 15)) * 16;
#pragma unroll
    for (int c = 0; c < 4; ++c) {
#pragma unroll
      for (int e = 0; e < 16; ++e) acc[c][e] *= linv;
      __builtin_amdgcn_raw_buffer_store_b128(__builtin_bit_cast(u32x4, pack8(acc[c], 0)), ors, ob + c * 1024, 0, 16);
      __builtin_amdgcn_raw_buffer_store_b128(__builtin_bit_cast(u32x4, pack8(acc[c], 1)), ors, ob + c * 1024 + 512, 0, 16);
    }
    SB();
    STAMP(26);
    wait_vm<0>();
    STAMP(27);
    {
      unsigned old = 0;
      if (lane == 0) old = __hip_atomic_fetch_add((LAS unsigned*)(size_t)(sbase + CTRL + 4 * W_OCNT), 1u, RLX_WG);
      old = __builtin_amdgcn_readfirstlane(old);
      if (old == 2u && lane == 0) __hip_atomic_store((gu64*)(ws + WS_FLAGS) + (size_t)(b * NH + h) * FLAG_STRIDE, tag + TAG_STEP, RLX_AGENT);
      SB();
    }
    wait_word(W_WT);
#pragma unroll
    for (int e = 0; e < 32; ++e) wf[e] = __builtin_amdgcn_raw_buffer_load_b128(ors, wt_off + lane * 16, e * 1024, 16);
    STAMP(28);
  }
  tag += TAG_STEP;
  const int lane = lane_id(), g4 = lane >> 4;
  {
    gu64* fp = (gu64*)(ws + WS_FLAGS) + (size_t)((rb >> 1) * NH + wid * 4 + (lane & 3)) * FLAG_STRIDE;
#pragma unroll 1
    for (unsigned sp = 0; sp < SPIN_CAP; ++sp) {
      const u64 f = __hip_atomic_load(fp, RLX_AGENT);
      if (__all(f == tag)) break;
      __builtin_amdgcn_s_sleep(1);
    }
    CFENCE();
  }
  STAMP(34);
  f32x4 oa[2][3];
#pragma unroll
  for (int nb = 0; nb < 2; ++nb)
#pragma unroll
    for (int u = 0; u < 3; ++u) oa[nb][u] = f32x4{};
  {
    const unsigned obase = ((rb * 3) * 128 + wid * 16) * 1024 + lane * 16;
    u32x4 of[2][3][4];
    auto ldo = [&](int hd) {
#pragma unroll
      for (int ks = 0; ks < 4; ++ks)
#pragma unroll
        for (int u = 0; u < 3; ++u) of[hd & 1][u][ks] = __builtin_amdgcn_raw_buffer_load_b128(ors, obase + (hd * 4 + ks) * 1024, u * 131072, 16);
    };
    ldo(0); ldo(1);
    SB();
#pragma unroll
    for (int hd = 0; hd < 4; ++hd) {
#pragma unroll
      for (int ks = 0; ks < 4; ++ks)
#pragma unroll
        for (int u = 0; u < 3; ++u)
#pragma unroll
          for (int nb = 0; nb < 2; ++nb) oa[nb][u] = MFMA16(__builtin_bit_cast(bf16x8, wf[(hd * 4 + ks) * 2 + nb]), __builtin_bit_cast(bf16x8, of[hd & 1][u][ks]), oa[nb][u]);
      SB();
      if (hd + 2 < 4) ldo(hd + 2);
      SB();
    }
  }
  STAMP(35);
  bar();
  LAS f32x4* red = (LAS f32x4*)(size_t)sbase;
#pragma unroll
  for (int nb = 0; nb < 2; ++nb)
#pragma unroll
    for (int u = 0; u < 3; ++u) red[(wid * 6 + nb * 3 + u) * 64 + lane] = oa[nb][u];
  bar();
  if (wid < 6) {
    f32x4 s = red[wid * 64 + lane];
#pragma unroll
    for (int w = 1; w < 8; ++w) s += red[(w * 6 + wid) * 64 + lane];
    const int nb = wid / 3, u = wid % 3;
    const int qrow = rb * 48 + u * 16 + (lane & 15), n = cb * 32 + nb * 16 + g4 * 4;
    u32x2 o = {cvtpk(s[0], s[1]), cvtpk(s[2], s[3])};
    *(u32x2*)(out + (size_t)qrow * DM + n) = o;
  } else if (wid == 7 && bid == 0) {
    bool ok = false;
#pragma unroll 1
    for (unsigned sp = 0; sp < SPIN_CAP && !ok; ++sp) {
      bool all = true;
#pragma unroll
      for (int i = 0; i < 4; ++i) all &= __hip_atomic_load((gu64*)(ws + WS_FLAGS) + (size_t)(i * 64 + lane) * FLAG_STRIDE, RLX_AGENT) == tag;
      ok = __all(all);
    }
    if (lane == 0) __hip_atomic_store((gu64*)(ws + WS_EPOCH), tag, RLX_AGENT);
  }
}

extern "C" void kernel_launch(void* const* d_in, const int* in_sizes, int n_in, void* d_out, int out_size, void* d_ws, size_t ws_size,
                              hipStream_t stream) {
  (void)hipFuncSetAttribute((const void*)fused_kernel, hipFuncAttributeMaxDynamicSharedMemorySize, LDS_BYTES);
  fused_kernel<<<256, 512, LDS_BYTES, stream>>>((const u16*)d_in[0], (const u16*)d_in[1], (const u16*)d_in[2], (const u16*)d_in[3],
                                                (const u16*)d_in[4], (u16*)d_out, (char*)d_ws);
}
```

```cpp
#include <hip/hip_runtime.h>
#include <hip/hip_bf16.h>
#include <utility>

#define LAS __attribute__((address_space(3)))
#define GAS __attribute__((address_space(1)))
#define DI __device__ __forceinline__
#define AI __attribute__((always_inline))
typedef __bf16 bf16x8 __attribute__((ext_vector_type(8)));
typedef __bf16 bf16x4 __attribute__((ext_vector_type(4)));
typedef __bf16 bf16x2 __attribute__((ext_vector_type(2)));
typedef float f32x16 __attribute__((ext_vector_type(16)));
typedef float f32x4 __attribute__((ext_vector_type(4)));
typedef float f32x2 __attribute__((ext_vector_type(2)));
typedef unsigned u32x4 __attribute__((ext_vector_type(4)));
typedef unsigned u32x2 __attribute__((ext_vector_type(2)));
typedef unsigned short u16;
typedef unsigned long long u64;
typedef GAS u64 gu64;
#define RLX_AGENT __ATOMIC_RELAXED, __HIP_MEMORY_SCOPE_AGENT
#define RLX_WG __ATOMIC_RELAXED, __HIP_MEMORY_SCOPE_WORKGROUP

#define K_NT 0
#define V_NT 1
#define WIN 3
#define WINQ 1
#define AUX_LD 0
#define AUX_ST 7
#define WF_EARLY 8
#define AUX_AT 10

constexpr int NB = 8, SQ = 96, DM = 512, NH = 32, SK = 512, DH = 128;
constexpr int NSLOT = 9, SLOTB = 16384, NT = 24;
constexpr int XREG = 6 * SLOTB, XAREA = 12288;
constexpr int CTRL = XREG + 5 * XAREA;
constexpr int SCR2 = CTRL + 1024;
constexpr int LDS_BYTES = 163840;
constexpr int W_FULL = 0, W_DONE = 4, W_OCNT = 8, W_TAG = 12, W_WT = 15;
constexpr size_t WS_EPOCH = 0, WS_FLAGS = 4096, WS_FLAG2 = 40960, WS_O = 65536, O_BYTES = (size_t)NB * 6 * 128 * 1024, WS_WT = WS_O + O_BYTES, WT_BYTES = (size_t)NH * DH * DM * 2;
constexpr int CODE_BYTES = 26112;
constexpr int FLAG_STRIDE = 16;
constexpr unsigned SPIN_CAP = 1u << 20;
constexpr u64 TAG_STEP = 0x9E3779B97F4A7C15ull;

constexpr int slot_tab[NT] = {0, 1, 2, 3, 4, 5, 0, 1, 2, 3, 4, 5, 0, 6, 1, 7, 8, 2, 3, 4, 5, 0, 6, 1};
constexpr int need_tab[NT] = {0, 0, 0, 0, 1, 2, 3, 0, 0, 4, 5, 6, 7, 0, 8, 0, 0, 9, 10, 11, 12, 13, 14, 15};
constexpr int nops(int c) { return c == 0 ? 10 : (c < 7 ? 7 : 4); }
constexpr int lag(int n) { return n <= 8 ? WINQ : (n - 8 + WINQ < WIN ? n - 8 + WINQ : WIN); }
constexpr int pubto(int n) { return n + 1 - lag(n) > 0 ? n + 1 - lag(n) : 0; }
constexpr int inflight(int n) { int s = 0; for (int i = pubto(n); i <= n; ++i) s += nops(i); return s; }
constexpr u64 pack_slots(int lo) { u64 v = 0; for (int i = 0; i < 16 && lo + i < NT; ++i) v |= (u64)slot_tab[lo + i] << (4 * i); return v; }
constexpr u64 SLOT_LO = pack_slots(0), SLOT_HI = pack_slots(16);
DI int slot_of(int c) { return (int)(((c < 16 ? SLOT_LO : SLOT_HI) >> (4 * (c & 15))) & 15); }

#define SB() __builtin_amdgcn_sched_barrier(0)
#define CFENCE() asm volatile("" ::: "memory")
template <int N> DI void wait_vm() { static_assert(N < 64, "vmcnt"); asm volatile("s_waitcnt vmcnt(%0)" ::"n"(N) : "memory"); }
DI void bar() { asm volatile("s_waitcnt lgkmcnt(0)\n\ts_barrier" ::: "memory"); }
DI unsigned cvtpk(float lo, float hi) { f32x2 v = {lo, hi}; bf16x2 b = __builtin_convertvector(v, bf16x2); return __builtin_bit_cast(unsigned, b); }
DI bf16x8 pack8(const f32x16& x, int s) {
  u32x4 w = {cvtpk(x[8 * s], x[8 * s + 1]), cvtpk(x[8 * s + 2], x[8 * s + 3]), cvtpk(x[8 * s + 4], x[8 * s + 5]), cvtpk(x[8 * s + 6], x[8 * s + 7])};
  return __builtin_bit_cast(bf16x8, w);
}
DI bf16x4 trread(unsigned a) { return __builtin_amdgcn_ds_read_tr16_b64_v4bf16((LAS bf16x4*)(size_t)a); }
DI bf16x8 tr2(unsigned a0, unsigned a1) { bf16x4 lo = trread(a0), hi = trread(a1); return __builtin_shufflevector(lo, hi, 0, 1, 2, 3, 4, 5, 6, 7); }
DI float swap32max(float v) { auto r = __builtin_amdgcn_permlane32_swap(__float_as_uint(v), __float_as_uint(v), false, false); return fmaxf(__uint_as_float(r[0]), __uint_as_float(r[1])); }
DI float swap32sum(float v) { auto r = __builtin_amdgcn_permlane32_swap(__float_as_uint(v), __float_as_uint(v), false, false); return __uint_as_float(r[0]) + __uint_as_float(r[1]); }
DI int lane_id() { int l; asm volatile("v_mbcnt_lo_u32_b32 %0, -1, 0\n\tv_mbcnt_hi_u32_b32 %0, -1, %0" : "=v"(l)); return l; }
DI float max3(float a, float b, float c) { float r; asm volatile("v_max3_f32 %0, %1, %2, %3" : "=v"(r) : "v"(a), "v"(b), "v"(c)); return r; }
DI unsigned opaque(unsigned v) { asm volatile("" : "+v"(v)); return v; }
#define MFMA32(a, b, c) __builtin_amdgcn_mfma_f32_32x32x16_bf16((a), (b), (c), 0, 0, 0)
#define MFMA16(a, b, c) __builtin_amdgcn_mfma_f32_16x16x32_bf16((a), (b), (c), 0, 0, 0)
#define SGB(mask, n) __builtin_amdgcn_sched_group_barrier((mask), (n), 0)
template <int I, int N, class F> DI void static_for(F&& f) { if constexpr (I < N) { f(std::integral_constant<int, I>{}); static_for<I + 1, N>(f); } }
template <int I> using IC = std::integral_constant<int, I>;

template <bool NTL> DI void dma4(unsigned lds, const char* g, unsigned v0, unsigned v1, unsigned v2, unsigned v3) {
  unsigned keep;
  if constexpr (NTL)
    asm volatile("s_mov_b32 %0, m0\n\ts_mov_b32 m0, %1\n\ts_nop 3\n\tglobal_load_lds_dwordx4 %3, %2 nt\n\t"
                 "s_add_u32 m0, m0, 0x400\n\ts_nop 0\n\tglobal_load_lds_dwordx4 %4, %2 nt\n\t"
                 "s_add_u32 m0, m0, 0x400\n\ts_nop 0\n\tglobal_load_lds_dwordx4 %5, %2 nt\n\t"
                 "s_add_u32 m0, m0, 0x400\n\ts_nop 0\n\tglobal_load_lds_dwordx4 %6, %2 nt\n\ts_mov_b32 m0, %0"
                 : "=&s"(keep) : "s"(lds), "s"(g), "v"(v0), "v"(v1), "v"(v2), "v"(v3) : "memory", "scc");
  else
    asm volatile("s_mov_b32 %0, m0\n\ts_mov_b32 m0, %1\n\ts_nop 3\n\tglobal_load_lds_dwordx4 %3, %2\n\t"
                 "s_add_u32 m0, m0, 0x400\n\ts_nop 0\n\tglobal_load_lds_dwordx4 %4, %2\n\t"
                 "s_add_u32 m0, m0, 0x400\n\ts_nop 0\n\tglobal_load_lds_dwordx4 %5, %2\n\t"
                 "s_add_u32 m0, m0, 0x400\n\ts_nop 0\n\tglobal_load_lds_dwordx4 %6, %2\n\ts_mov_b32 m0, %0"
                 : "=&s"(keep) : "s"(lds), "s"(g), "v"(v0), "v"(v1), "v"(v2), "v"(v3) : "memory", "scc");
}

DI void dma3(unsigned lds, const char* g, unsigned v0, unsigned v1, unsigned v2) {
  unsigned keep;
  asm volatile("s_mov_b32 %0, m0\n\ts_mov_b32 m0, %1\n\ts_nop 3\n\tglobal_load_lds_dwordx4 %3, %2\n\t"
               "s_add_u32 m0, m0, 0x400\n\ts_nop 0\n\tglobal_load_lds_dwordx4 %4, %2\n\t"
               "s_add_u32 m0, m0, 0x400\n\ts_nop 0\n\tglobal_load_lds_dwordx4 %5, %2\n\ts_mov_b32 m0, %0"
               : "=&s"(keep) : "s"(lds), "s"(g), "v"(v0), "v"(v1), "v"(v2) : "memory", "scc");
}

#define STAMP(i) do {} while (0)
#define STAMPL(i) do {} while (0)
#define CSTAMP(i) do {} while (0)

__global__ __launch_bounds__(512, 2) void fused_kernel(const u16* __restrict__ x, const u16* __restrict__ kc, const u16* __restrict__ vc,
                                                      const u16* __restrict__ wq, const u16* __restrict__ wo, u16* __restrict__ out, char* ws) {
  extern __shared__ __attribute__((aligned(16))) char smem[];
  const char* pc0;
  asm volatile("s_getpc_b64 %0" : "=s"(pc0));
  const int tid = threadIdx.x;
  const int wid = __builtin_amdgcn_readfirstlane(tid >> 6);
  const int bid = blockIdx.x, xcd = bid & 7, idx = bid >> 3;
  const int h = xcd * 4 + (idx & 3), b = idx >> 2;
  const int rb = 4 * (xcd >> 1) + (idx >> 3), cb = 8 * (xcd & 1) + (idx & 7);
  const int rot = (b + 2 * (idx & 3)) & 7;
  const unsigned sbase = (unsigned)(size_t)(LAS char*)smem;
  volatile LAS unsigned* ctl = (volatile LAS unsigned*)(size_t)(sbase + CTRL);
  u32x4 wf[32];
  u64 tag;
  const __amdgpu_buffer_rsrc_t ors = __builtin_amdgcn_make_buffer_rsrc(ws + WS_O, 0, (int)(O_BYTES + WT_BYTES), 0x00020000);
  const unsigned wt_off = (unsigned)O_BYTES + (unsigned)(cb * 128 + wid * 16) * 2048;
  unsigned done_seen = 0;
  auto wait_done = [&](unsigned need) AI {
    if (done_seen < need) {
#pragma unroll 1
      for (unsigned sp = 0; sp < SPIN_CAP; ++sp) {
        const u32x4 d = *(volatile LAS u32x4*)(size_t)(sbase + CTRL + 4 * W_DONE);
        done_seen = __builtin_amdgcn_readfirstlane(min(d.x, min(d.y, d.z)));
        if (done_seen >= need) break;
        __builtin_amdgcn_s_sleep(1);
      }
    }
    CFENCE();
  };
  auto wait_word = [&](int w) AI {
#pragma unroll 1
    for (unsigned sp = 0; sp < SPIN_CAP; ++sp) { if (__builtin_amdgcn_readfirstlane(ctl[w]) != 0u) break; __builtin_amdgcn_s_sleep(1); }
    CFENCE();
  };
  auto wait_landed = [&](unsigned n) AI {
#pragma unroll 1
    for (unsigned sp = 0; sp < SPIN_CAP; ++sp) { if ((__builtin_amdgcn_readfirstlane(ctl[W_FULL]) & 255u) >= n) break; __builtin_amdgcn_s_sleep(2); }
    CFENCE();
  };
  auto get_tag = [&]() AI { wait_word(W_TAG + 2); tag = (u64)ctl[W_TAG] | ((u64)ctl[W_TAG + 1] << 32); };

  if (__builtin_expect(wid >= 4, 1)) {
    const int lane = tid & 63, q = wid - 4;
    if (q == 3) {
      const unsigned co = min((unsigned)(idx * 8 + (lane & 7)) * 128u, (unsigned)CODE_BYTES);
      unsigned keep;
      asm volatile("s_mov_b32 %0, m0\n\ts_mov_b32 m0, %1\n\ts_nop 3\n\tglobal_load_lds_dword %3, %2\n\ts_mov_b32 m0, %0"
                   : "=&s"(keep) : "s"(sbase + SCR2 + 2048 + 512), "s"(pc0), "v"(co) : "memory");
    }
    const int r7 = lane >> 3, sl = lane & 7;
    const unsigned c0 = (sl ^ (((r7 & 2) << 1) | (r7 >> 2))) * 16, c1 = c0 ^ 32;
    const unsigned kv0 = r7 * 256 + c0, kv2 = (8 + r7) * 256 + c1, w0 = r7 * 8192 + c0, w2 = (8 + r7) * 8192 + c1;
    const char* kb = (const char*)kc + (size_t)(b * NH + h) * SK * DH * 2 + q * 4096;
    const char* vb = (const char*)vc + (size_t)(b * NH + h) * SK * DH * 2 + q * 4096;
    const char* wb = (const char*)wq + h * DH * 2 + q * 16 * 8192;
    const unsigned ldsq = sbase + q * 4096;
    volatile LAS unsigned char* fullb = (volatile LAS unsigned char*)(size_t)(sbase + CTRL + 4 * W_FULL);
    const char* xb = (const char*)x + (size_t)(b * SQ + q * 24) * (DM * 2);
    const unsigned xc = c0 ^ ((q & 1) * 32), x0 = r7 * 1024 + xc, x1 = (8 + r7) * 1024 + (xc ^ 32), x2 = x0 + 16 * 1024;
    auto ldx = [&](int j) AI { dma3(sbase + XREG + (j % 5) * XAREA + q * 3072, xb + ((j + rot) & 7) * 128, x0, x1, x2); };
    static_for<0, NT>([&](auto N) AI {
      constexpr int c = N.value;
      if constexpr (need_tab[c] > 0) wait_done(need_tab[c]);
      const unsigned lds = ldsq + slot_tab[c] * SLOTB;
      if constexpr (c < 8) {
        dma4<false>(lds, wb + (size_t)((c + rot) & 7) * 64 * 8192, w0, w0 + 128, w2, w2 + 128);
        if constexpr (c == 0) ldx(0);
        if constexpr (c < 7) ldx(c + 1);
        if constexpr (c == 0) bar();
      } else {
        constexpr bool isk = (c == 8) || ((c & 1) && c != 23);
        constexpr int ti = c == 8 ? 0 : (c == 23 ? 7 : ((c & 1) ? (c - 7) >> 1 : (c - 10) >> 1));
        if constexpr (isk) dma4<K_NT != 0>(lds, kb + ti * SLOTB, kv0, kv0 + 128, kv2, kv2 + 128);
        else dma4<V_NT != 0>(lds, vb + ti * SLOTB, kv0, kv0 + 128, kv2, kv2 + 128);
      }
      if constexpr (c > 0 && pubto(c) > pubto(c - 1)) { wait_vm<inflight(c)>(); fullb[q] = (unsigned char)pubto(c); CFENCE(); STAMP(pubto(c)); }
    });
    static_for<0, WIN>([&](auto I) AI {
      constexpr int i = I.value;
      wait_vm<4 * (WIN - 1 - i)>(); fullb[q] = (unsigned char)(NT - WIN + i + 1); CFENCE(); STAMP(1 + NT - WIN + i);
    });
    wait_word(W_WT);
#pragma unroll
    for (int e = 0; e < WF_EARLY; ++e) wf[e] = __builtin_amdgcn_raw_buffer_load_b128(ors, wt_off + lane * 16, e * 1024, 16);
    SB();
    wait_done(NT);
#pragma unroll
    for (int e = WF_EARLY; e < 32; ++e) wf[e] = __builtin_amdgcn_raw_buffer_load_b128(ors, wt_off + lane * 16, e * 1024, 16);
    get_tag();
    STAMP(33);
  } else if (wid == 3) {
    const int lane = tid & 63;
    const int ks = bid >> 1, half = bid & 1;
    {
      const char* src = (const char*)wo + (size_t)(ks * 32 + (lane >> 3)) * 1024 + half * 512 + (lane & 7) * 16;
      u32x4 raw[16];
      if (lane < 16) ctl[lane] = 0u;
      bar();
      if (AUX_LD > 0) wait_landed(AUX_LD);
      tag = __hip_atomic_load((gu64*)(ws + WS_EPOCH), RLX_AGENT);
#pragma unroll
      for (int j = 0; j < 4; ++j)
#pragma unroll
        for (int a = 0; a < 4; ++a) raw[4 * j + a] = __builtin_nontemporal_load((const u32x4*)(src + a * 8192 + j * 128));
      SB();
      const int g4 = lane >> 4, qq = (lane & 15) >> 2, p = lane & 3;
      const unsigned sc = sbase + SCR2, ta = sc + 128 * (4 * (g4 & 1) + qq) + 32 * (g4 >> 1) + 8 * p;
      const unsigned dst = (unsigned)O_BYTES + (unsigned)(half * 8 * 128 + ks) * 2048 + (lane >> 5) * 1024 + (lane & 31) * 16;
#pragma unroll
      for (int ps = 0; ps < 8; ++ps) {
        *(LAS u32x4*)(size_t)(sc + lane * 16) = raw[2 * ps];
        *(LAS u32x4*)(size_t)(sc + 1024 + lane * 16) = raw[2 * ps + 1];
        CFENCE();
#pragma unroll
        for (int m = 0; m < 2; ++m) raw[2 * ps + m] = __builtin_bit_cast(u32x4, tr2(ta + m * 64, ta + m * 64 + 1024));
        CFENCE();
      }
      wait_landed(AUX_ST);
#pragma unroll
      for (int e = 0; e < 16; ++e) __builtin_amdgcn_raw_buffer_store_b128(raw[e], ors, dst + ((e >> 1) & 1) * 512, (2 * (e >> 2) + (e & 1)) * 128 * 2048, 16);
      ctl[W_TAG] = (unsigned)tag; ctl[W_TAG + 1] = (unsigned)(tag >> 32);
      CFENCE();
      ctl[W_TAG + 2] = 1u;
      wait_vm<0>();
      if (lane == 0) __hip_atomic_store((gu64*)(ws + WS_FLAG2) + half * 128 + ks, tag + TAG_STEP, RLX_AGENT);
    }
    STAMP(1);
    wait_done(AUX_AT);
    {
      gu64* fp = (gu64*)(ws + WS_FLAG2) + (cb >> 3) * 128 + lane;
#pragma unroll 1
      for (unsigned sp = 0; sp < SPIN_CAP; ++sp) {
        const u64 f0 = __hip_atomic_load(fp, RLX_AGENT), f1 = __hip_atomic_load(fp + 64, RLX_AGENT);
        if (__all(f0 == tag + TAG_STEP && f1 == tag + TAG_STEP)) break;
        __builtin_amdgcn_s_sleep(4);
      }
      CFENCE();
      ctl[W_WT] = 1u;
      CFENCE();
    }
    STAMP(2);
    wait_landed(NT - WIN);
#pragma unroll
    for (int e = 0; e < 32; ++e) wf[e] = __builtin_amdgcn_raw_buffer_load_b128(ors, wt_off + lane * 16, e * 1024, 16);
    STAMP(33);
  } else {
    int lane = tid & 63;
    int r = lane & 31, hh = lane >> 5, blk = (lane >> 4) & 1, qq = (lane & 15) >> 2, p = lane & 3;
    const int ps = ((p & 1) << 1) | (p >> 1);
    bar();
    unsigned fl;
    const unsigned cta = opaque(sbase + CTRL), dna = opaque(sbase + CTRL + 4 * (W_DONE + wid));
    unsigned dnv = 0;
    auto lacking = [](unsigned v, int n) AI { return (((unsigned)__builtin_amdgcn_readfirstlane(v) - (unsigned)(n + 1) * 0x01010101u) & 0x80808080u) != 0u; };
    auto peek = [&]() AI { fl = *(volatile LAS unsigned*)(size_t)(cta + 4 * W_FULL); };
    auto wait_full = [&](int n) AI {
      if (__builtin_expect(lacking(fl, n), 0)) {
#pragma unroll 1
        for (unsigned sp = 0; sp < SPIN_CAP; ++sp) { if (!lacking(*(volatile LAS unsigned*)(size_t)(cta + 4 * W_FULL), n)) break; }
      }
      CFENCE();
    };
    auto release = [&]() AI { CFENCE(); dnv += 1; asm volatile("" : "+v"(dnv)); *(volatile LAS unsigned*)(size_t)dna = dnv; CFENCE(); };
    const unsigned wa = sbase + 2048 * hh + 128 * qq + 16 * ((2 * blk + (ps >> 1)) ^ (((qq & 2) << 1) | (2 * hh))) + 8 * (ps & 1);
    const unsigned va = sbase + 128 * (4 * hh + qq) + 16 * ((2 * blk + (p >> 1)) ^ (((qq & 2) << 1) | hh)) + 8 * (p & 1);
    const unsigned fr = 128 * (r & 7) + 16 * (hh ^ (((r & 2) << 1) | ((r >> 2) & 3)));
    const unsigned ka = sbase + 2048 * (r >> 3) + fr;
    const unsigned xa = sbase + XREG + 1024 * (4 * wid + (r >> 3)) + fr;
    f32x16 acc[4];
    bf16x8 FR[8];
    unsigned ab[4], kb4[4];
    auto setab = [&](unsigned (&d)[4], unsigned a, unsigned x1, unsigned x2) AI { d[0] = opaque(a); d[1] = opaque(a ^ x1); d[2] = opaque(a ^ x2); d[3] = opaque(a ^ x1 ^ x2); };
    auto seta = [&](unsigned (&d)[4], unsigned a, int j) AI { d[j] = opaque(j == 0 ? a : d[0] ^ (32 * j)); };
    auto rdw = [&](int i, int so) AI { const int ks = i >> 2, c = i & 3, o = so + 4096 * ks + 1024 * (c >> 1); FR[i & 7] = tr2(ab[2 * (c & 1)] + o, ab[2 * (c & 1) + 1] + o + 512); };
    auto rdv = [&](int i) AI { const int ks = i >> 2, c = i & 3, o = 4096 * ks + 1024 * (c >> 1); FR[i & 7] = tr2(ab[2 * (c & 1)] + o, ab[2 * (c & 1) + 1] + o + 2048); };
    auto rdk = [&](int i) AI { FR[i & 7] = *(LAS bf16x8*)(size_t)(kb4[i & 3] + 1024 * ((i >> 2) & 1) + 8192 * (i >> 3)); };
    auto ready = [&](int c) AI { wait_full(c); STAMPL(1 + c); SB(); };
    {
      bf16x8 XF[4];
      unsigned xb4[4] = {opaque(xa), opaque(xa ^ 32), opaque(xa ^ 64), opaque(xa ^ 96)};
      auto rdx = [&](int tile, int ks) AI { XF[ks] = *(LAS bf16x8*)(size_t)(xb4[ks] + (tile % 5) * XAREA); };
      setab(ab, wa, 16, 64);
      setab(kb4, ka + slot_tab[8] * SLOTB, 32, 64);
      peek(); wait_full(0); STAMP(1); CSTAMP(29);
      rdx(0, 0); rdx(0, 1);
#pragma unroll
      for (int i = 0; i < 6; ++i) { rdw(i ^ 1, 0); SB(); }
      SB();
      static_for<0, 8>([&](auto TL) AI {
        constexpr int tile = TL.value;
        static_for<0, 16>([&](auto I) AI {
          constexpr int i = I.value;
          if constexpr (i == 10) { ready(tile + 1); if constexpr (tile == 3 || tile == 5) { setab(ab, wa + (tile == 3 ? 65536 : 0), 16, 64); SB(); } }
          acc[i & 3] = MFMA32(FR[i & 7], XF[i >> 2], (tile == 0 && i < 4) ? f32x16{} : acc[i & 3]);
          SB();
          if constexpr ((i & 1) == 0) {
            if constexpr (i < 10) { rdw(i + 7, (slot_tab[tile] & 3) * SLOTB); SB(); rdw(i + 6, (slot_tab[tile] & 3) * SLOTB); }
            else if constexpr (tile < 7) { rdw(i - 9, (slot_tab[tile + 1] & 3) * SLOTB); SB(); rdw(i - 10, (slot_tab[tile + 1] & 3) * SLOTB); }
            else { rdk(i - 9); SB(); rdk(i - 10); }
          }
          if constexpr (i == 1) rdx(tile, 2);
          if constexpr (i == 5) rdx(tile, 3);
          if constexpr (i == 7) peek();
          if constexpr (i == 9) release();
          if constexpr (i == 11 && tile < 7) rdx(tile + 1, 0);
          if constexpr (i == 13 && tile < 7) rdx(tile + 1, 1);
          SB();
        });
      });
    }
    STAMP(9); CSTAMP(30);
    const float CS = 0.08838834764831845f * 1.4426950408889634f;
    bf16x8 qf[8];
#pragma unroll
    for (int c = 0; c < 4; ++c) { acc[c] *= CS; qf[2 * c] = pack8(acc[c], 0); qf[2 * c + 1] = pack8(acc[c], 1); }
#pragma unroll
    for (int c = 0; c < 4; ++c) acc[c] = f32x16{};
    float l = 0.f, alpha = 1.f, rs0 = 0.f, rs1 = 0.f, mxa, mxb;
    bool pend = false;
    f32x16 s0, s1, negm;
    float e0[16], e1[16];
    unsigned pw[16];
    auto pin_acc = [&]() AI { asm volatile("" : "+v"(acc[0]), "+v"(acc[1]), "+v"(acc[2]), "+v"(acc[3])); };
#define PIN(v) asm volatile("" : "+v"(v))
    auto ex0 = [&](int i) AI { e0[i] = __builtin_amdgcn_exp2f(s0[i]); PIN(e0[i]); };
    auto ex1 = [&](int i) AI { e1[i] = __builtin_amdgcn_exp2f(s1[i]); PIN(e1[i]); };
    auto ad0 = [&](int i) AI { rs0 += e0[i]; PIN(rs0); rs1 += e0[i + 1]; PIN(rs1); };
    auto ad1 = [&](int i) AI { rs0 += e1[i]; PIN(rs0); rs1 += e1[i + 1]; PIN(rs1); };
    auto pk0 = [&](int w) AI { pw[w] = cvtpk(e0[2 * w], e0[2 * w + 1]); PIN(pw[w]); };
    auto pk1 = [&](int w) AI { pw[8 + w] = cvtpk(e1[2 * w], e1[2 * w + 1]); PIN(pw[8 + w]); };
    auto pfrag = [&](int k) AI { u32x4 v = {pw[4 * k], pw[4 * k + 1], pw[4 * k + 2], pw[4 * k + 3]}; return __builtin_bit_cast(bf16x8, v); };
    auto mx0 = [&](int i) AI { float& m = (i & 1) ? mxb : mxa; m = i < 2 ? fmaxf(s0[2 * i], s0[2 * i + 1]) : fmaxf(fmaxf(m, s0[2 * i]), s0[2 * i + 1]); PIN(m); };
    auto mx1 = [&](int i) AI { float& m = (i & 1) ? mxb : mxa; m = fmaxf(fmaxf(m, s1[2 * i]), s1[2 * i + 1]); PIN(m); };
    auto shift_ref = [&](float d) AI {
#pragma unroll
      for (int e = 0; e < 16; ++e) { s0[e] -= d; s1[e] -= d; negm[e] -= d; }
    };
    auto decide = [&]() AI {
      const float mx = fmaxf(mxa, mxb);
      if (__builtin_expect(__any(mx > 8.f), 0)) {
        const float d = fmaxf(swap32max(mx), 0.f);
        shift_ref(d);
        alpha = __builtin_amdgcn_exp2f(-d);
        l = (l + rs0 + rs1) * alpha; rs0 = 0.f; rs1 = 0.f;
        pend = true;
      }
      SB();
    };
    auto rescale = [&]() AI {
      if (__builtin_expect(pend, 0)) {
#pragma unroll
        for (int cc = 0; cc < 4; ++cc) acc[cc] *= alpha;
        pend = false;
      }
      SB();
    };
    negm = f32x16{};
    static_for<0, 16>([&](auto I) AI {
      constexpr int i = I.value;
      if constexpr (i == 10) { ready(9); setab(kb4, ka + slot_tab[9] * SLOTB, 32, 64); SB(); }
      if constexpr (i < 8) s0 = MFMA32(FR[i & 7], qf[i & 7], (i == 0 ? negm : s0)); else s1 = MFMA32(FR[i & 7], qf[i & 7], (i == 8 ? negm : s1));
        if constexpr (i < 8) PIN(s0); else PIN(s1);
      SB();
      if constexpr ((i & 1) == 0) { rdk((i + 7) & 15); SB(); rdk((i + 6) & 15); }
      if constexpr (i == 7) peek();
      if constexpr (i == 9) release();
      if constexpr (i >= 11 && i < 15) { mx0(2 * (i - 11)); mx0(2 * (i - 11) + 1); }
      SB();
    });
#pragma unroll
    for (int i = 0; i < 8; ++i) mx1(i);
    shift_ref(swap32max(fmaxf(mxa, mxb)));
#pragma unroll
    for (int i = 0; i < 16; ++i) ex0(i);
#pragma unroll
    for (int i = 0; i < 8; ++i) { ad0(2 * i); pk0(i); }
#pragma unroll
    for (int i = 0; i < 4; ++i) ex1(i);
    SB();
    u64 seq = pack_slots(10);
#pragma unroll 1
    for (int c = 9; c < 23; c += 2) {
      rescale();
      static_for<0, 16>([&](auto I) AI {
        constexpr int i = I.value;
        if constexpr (i == 10) ready(c + 1);
        if constexpr (i < 8) s0 = MFMA32(FR[i & 7], qf[i & 7], (i == 0 ? negm : s0)); else s1 = MFMA32(FR[i & 7], qf[i & 7], (i == 8 ? negm : s1));
        if constexpr (i < 8) PIN(s0); else PIN(s1);
        SB();
        if constexpr ((i & 1) == 0) { if constexpr (i < 10) { rdk(i + 7); SB(); rdk(i + 6); } else { rdv(i - 9); SB(); rdv(i - 10); } }
        if constexpr (i == 0) { ex1(4); pk1(0); seta(ab, va + ((unsigned)seq & 15u) * SLOTB, 0); }
        if constexpr (i == 1) { ex1(5); ex1(6); ad1(0); }
        if constexpr (i == 2) { ex1(7); pk1(1); seta(ab, 0, 1); }
        if constexpr (i == 3) { ex1(8); ex1(9); ad1(2); }
        if constexpr (i == 4) { ex1(10); pk1(2); seta(ab, 0, 2); }
        if constexpr (i == 5) { ex1(11); ex1(12); ad1(4); }
        if constexpr (i == 6) { ex1(13); pk1(3); seta(ab, 0, 3); }
        if constexpr (i == 7) { ex1(14); ex1(15); peek(); }
        if constexpr (i == 8) { ad1(6); pk1(4); }
        if constexpr (i == 9) { release(); }
        if constexpr (i == 10) { pk1(5); }
        if constexpr (i == 11) { mx0(0); mx0(1); ad1(8); }
        if constexpr (i == 12) { pk1(6); }
        if constexpr (i == 13) { mx0(2); mx0(3); ad1(10); mx0(4); mx0(5); }
        if constexpr (i == 14) { pk1(7); }
        if constexpr (i == 15) { mx0(6); mx0(7); ad1(12); ad1(14); }
        SB();
      });
      static_for<0, 16>([&](auto I) AI {
        constexpr int i = I.value;
        if constexpr (i == 10) ready(c == 21 ? 21 : c + 2);
        acc[i & 3] = MFMA32(FR[i & 7], pfrag(i >> 2), acc[i & 3]);
        PIN(acc[i & 3]);
        SB();
        if constexpr ((i & 1) == 0) { if constexpr (i < 10) { rdv(i + 7); SB(); rdv(i + 6); } else { rdk(i - 9); SB(); rdk(i - 10); } }
        if constexpr (i == 0) { seta(kb4, ka + ((unsigned)(seq >> 4) & 15u) * SLOTB, 0); seq >>= 8; }
        if constexpr (i == 1) { mx1(0); mx1(1); mx1(2); mx1(3); mx1(4); mx1(5); }
        if constexpr (i == 2) { mx1(6); mx1(7); }
        if constexpr (i == 3) { decide(); ex0(0); ex0(1); }
        if constexpr (i == 4) { seta(kb4, 0, 1); }
        if constexpr (i == 5) { ex0(2); ex0(3); ad0(0); }
        if constexpr (i == 6) { ex0(4); seta(kb4, 0, 2); }
        if constexpr (i == 7) { ex0(5); ex0(6); ad0(2); peek(); }
        if constexpr (i == 8) { ex0(7); seta(kb4, 0, 3); }
        if constexpr (i == 9) { release(); ex0(8); }
        if constexpr (i == 10) { ex0(9); ad0(4); pk0(0); }
        if constexpr (i == 11) { ex0(10); ex0(11); ad0(6); pk0(1); pk0(2); }
        if constexpr (i == 12) { ex0(12); ex0(13); pk0(3); pk0(4); }
        if constexpr (i == 13) { ex0(14); ex0(15); ad0(8); pk0(5); }
        if constexpr (i == 14) { ex1(0); ex1(1); ad0(10); pk0(6); }
        if constexpr (i == 15) { ex1(2); ex1(3); ad0(12); ad0(14); pk0(7); }
        SB();
      });
    }
    rescale();
#pragma unroll
    for (int i = 4; i < 16; ++i) ex1(i);
#pragma unroll
    for (int i = 0; i < 8; ++i) { ad1(2 * i); pk1(i); }
    const float linv = 1.f / swap32sum(l + rs0 + rs1);
    get_tag();
    peek();
    wait_full(23);
    STAMP(24); CSTAMP(31);
    setab(ab, va + slot_tab[23] * SLOTB, 32, 64);
#pragma unroll
    for (int i = 0; i < 6; ++i) rdv(i);
    SB();
    static_for<0, 16>([&](auto I) AI {
      constexpr int i = I.value;
      acc[i & 3] = MFMA32(FR[i & 7], pfrag(i >> 2), acc[i & 3]);
      SB();
      if constexpr ((i & 1) == 0 && i < 10) { rdv(i + 7); SB(); rdv(i + 6); }
      SB();
    });
    STAMP(25);
    lane = lane_id(); r = lane & 31; hh = lane >> 5;
    const int u = 2 * wid + (r >> 4);
    const unsigned ob = (((b * 6 + u) * 128 + h * 4) * 64 + hh * 16 + (r & 15)) * 16;
#pragma unroll
    for (int c = 0; c < 4; ++c) {
#pragma unroll
      for (int e = 0; e < 16; ++e) acc[c][e] *= linv;
      __builtin_amdgcn_raw_buffer_store_b128(__builtin_bit_cast(u32x4, pack8(acc[c], 0)), ors, ob + c * 1024, 0, 16);
      __builtin_amdgcn_raw_buffer_store_b128(__builtin_bit_cast(u32x4, pack8(acc[c], 1)), ors, ob + c * 1024 + 512, 0, 16);
    }
    SB();
    release();
    STAMP(26);
    wait_vm<0>();
    STAMP(27);
    {
      unsigned old = 0;
      if (lane == 0) old = __hip_atomic_fetch_add((LAS unsigned*)(size_t)(sbase + CTRL + 4 * W_OCNT), 1u, RLX_WG);
      old = __builtin_amdgcn_readfirstlane(old);
      if (old == 2u && lane == 0) __hip_atomic_store((gu64*)(ws + WS_FLAGS) + (size_t)(b * NH + h) * FLAG_STRIDE, tag + TAG_STEP, RLX_AGENT);
      SB();
    }
    wait_word(W_WT);
#pragma unroll
    for (int e = 0; e < 32; ++e) wf[e] = __builtin_amdgcn_raw_buffer_load_b128(ors, wt_off + lane * 16, e * 1024, 16);
    STAMP(28);
  }
  tag += TAG_STEP;
  const int lane = lane_id(), g4 = lane >> 4;
  {
    gu64* fp = (gu64*)(ws + WS_FLAGS) + (size_t)((rb >> 1) * NH + wid * 4 + (lane & 3)) * FLAG_STRIDE;
#pragma unroll 1
    for (unsigned sp = 0; sp < SPIN_CAP; ++sp) {
      const u64 f = __hip_atomic_load(fp, RLX_AGENT);
      if (__all(f == tag)) break;
      __builtin_amdgcn_s_sleep(1);
    }
    CFENCE();
  }
  STAMP(34);
  f32x4 oa[2][3];
#pragma unroll
  for (int nb = 0; nb < 2; ++nb)
#pragma unroll
    for (int u = 0; u < 3; ++u) oa[nb][u] = f32x4{};
  {
    const unsigned obase = ((rb * 3) * 128 + wid * 16) * 1024 + lane * 16;
    u32x4 of[2][3][4];
    auto ldo = [&](int hd) AI {
#pragma unroll
      for (int ks = 0; ks < 4; ++ks)
#pragma unroll
        for (int u = 0; u < 3; ++u) of[hd & 1][u][ks] = __builtin_amdgcn_raw_buffer_load_b128(ors, obase + (hd * 4 + ks) * 1024, u * 131072, 16);
    };
    ldo(0); ldo(1);
    SB();
#pragma unroll
    for (int hd = 0; hd < 4; ++hd) {
#pragma unroll
      for (int ks = 0; ks < 4; ++ks)
#pragma unroll
        for (int u = 0; u < 3; ++u)
#pragma unroll
          for (int nb = 0; nb < 2; ++nb) oa[nb][u] = MFMA16(__builtin_bit_cast(bf16x8, wf[(hd * 4 + ks) * 2 + nb]), __builtin_bit_cast(bf16x8, of[hd & 1][u][ks]), oa[nb][u]);
      SB();
      if (hd + 2 < 4) ldo(hd + 2);
      SB();
    }
  }
  STAMP(35);
  bar();
  LAS f32x4* red = (LAS f32x4*)(size_t)sbase;
#pragma unroll
  for (int nb = 0; nb < 2; ++nb)
#pragma unroll
    for (int u = 0; u < 3; ++u) red[(wid * 6 + nb * 3 + u) * 64 + lane] = oa[nb][u];
  bar();
  if (wid < 6) {
    f32x4 s = red[wid * 64 + lane];
#pragma unroll
    for (int w = 1; w < 8; ++w) s += red[(w * 6 + wid) * 64 + lane];
    const int nb = wid / 3, u = wid % 3;
    const int qrow = rb * 48 + u * 16 + (lane & 15), n = cb * 32 + nb * 16 + g4 * 4;
    u32x2 o = {cvtpk(s[0], s[1]), cvtpk(s[2], s[3])};
    *(u32x2*)(out + (size_t)qrow * DM + n) = o;
  } else if (wid == 7 && bid == 0) {
    bool ok = false;
#pragma unroll 1
    for (unsigned sp = 0; sp < SPIN_CAP && !ok; ++sp) {
      bool all = true;
#pragma unroll
      for (int i = 0; i < 4; ++i) all &= __hip_atomic_load((gu64*)(ws + WS_FLAGS) + (size_t)(i * 64 + lane) * FLAG_STRIDE, RLX_AGENT) == tag;
      ok = __all(all);
    }
    if (lane == 0) __hip_atomic_store((gu64*)(ws + WS_EPOCH), tag, RLX_AGENT);
  }
}

extern "C" void kernel_launch(void* const* d_in, const int* in_sizes, int n_in, void* d_out, int out_size, void* d_ws, size_t ws_size,
                              hipStream_t stream) {
  (void)hipFuncSetAttribute((const void*)fused_kernel, hipFuncAttributeMaxDynamicSharedMemorySize, LDS_BYTES);
  fused_kernel<<<256, 512, LDS_BYTES, stream>>>((const u16*)d_in[0], (const u16*)d_in[1], (const u16*)d_in[2], (const u16*)d_in[3],
                                                (const u16*)d_in[4], (u16*)d_out, (char*)d_ws);
}
```

```cpp
#include <hip/hip_runtime.h>
#include <hip/hip_bf16.h>
#include <utility>

#define LAS __attribute__((address_space(3)))
#define GAS __attribute__((address_space(1)))
#define DI __device__ __forceinline__
#define AI __attribute__((always_inline))
typedef __bf16 bf16x8 __attribute__((ext_vector_type(8)));
typedef __bf16 bf16x4 __attribute__((ext_vector_type(4)));
typedef __bf16 bf16x2 __attribute__((ext_vector_type(2)));
typedef float f32x16 __attribute__((ext_vector_type(16)));
typedef float f32x4 __attribute__((ext_vector_type(4)));
typedef float f32x2 __attribute__((ext_vector_type(2)));
typedef unsigned u32x4 __attribute__((ext_vector_type(4)));
typedef unsigned u32x2 __attribute__((ext_vector_type(2)));
typedef unsigned short u16;
typedef unsigned long long u64;
typedef GAS u64 gu64;
#define RLX_AGENT __ATOMIC_RELAXED, __HIP_MEMORY_SCOPE_AGENT
#define RLX_WG __ATOMIC_RELAXED, __HIP_MEMORY_SCOPE_WORKGROUP

#define K_NT 1
#define V_NT 1
#define WIN 3
#define WINQ 1
#define AUX_LD 3
#define PACE_A 600
#define PACE_T 54
#define AUX_ST 0
#define WF_EARLY 0
#define AUX_AT 10

constexpr int NB = 8, SQ = 96, DM = 512, NH = 32, SK = 512, DH = 128;
constexpr int NSLOT = 9, SLOTB = 16384, NT = 24;
constexpr int XREG = 6 * SLOTB, XAREA = 12288;
constexpr int CTRL = XREG + 5 * XAREA;
constexpr int SCR2 = CTRL + 1024;
constexpr int LDS_BYTES = 163840;
constexpr int W_FULL = 0, W_DONE = 4, W_OCNT = 8, W_TAG = 12, W_WT = 15;
constexpr size_t WS_EPOCH = 0, WS_FLAGS = 4096, WS_FLAG2 = 40960, WS_O = 65536, O_BYTES = (size_t)NB * 6 * 128 * 1024, WS_WT = WS_O + O_BYTES, WT_BYTES = (size_t)NH * DH * DM * 2;
constexpr int CODE_BYTES = 26112;
constexpr int FLAG_STRIDE = 16;
constexpr unsigned SPIN_CAP = 1u << 20;
constexpr u64 TAG_STEP = 0x9E3779B97F4A7C15ull;

constexpr int slot_tab[NT] = {0, 1, 2, 3, 0, 1, 2, 3, 4, 5, 0, 1, 2, 6, 3, 7, 8, 4, 5, 0, 1, 2, 6, 3};
constexpr int need_tab[NT] = {0, 0, 0, 0, 1, 2, 3, 4, 0, 0, 5, 6, 7, 0, 8, 0, 0, 9, 10, 11, 12, 13, 14, 15};
constexpr int tile_at(int n) { return n < 4 ? n : (n < 6 ? n + 4 : (n < 10 ? n - 2 : n)); }
constexpr int order_of(int c) { return c < 4 ? c : (c < 8 ? c + 2 : (c < 10 ? c - 4 : c)); }
constexpr int nops(int n) { return n == 0 ? 10 : (tile_at(n) < 7 ? 7 : 4); }
constexpr int pub_tab[13] = {1, 1, 2, 3, 3, 4, 4, 7, 8, 10, 10, 10, 10};
constexpr int pubto(int n) { return n < 10 ? pub_tab[n] : (n + 1 - WIN > 10 ? n + 1 - WIN : 10); }
constexpr int inflight(int p, int n) { int s = (p == 1 ? 3 : 0); for (int i = p; i <= n; ++i) s += nops(i); return s; }
constexpr u64 pack_slots(int lo) { u64 v = 0; for (int i = 0; i < 16 && lo + i < NT; ++i) v |= (u64)slot_tab[lo + i] << (4 * i); return v; }
constexpr u64 SLOT_LO = pack_slots(0), SLOT_HI = pack_slots(16);
DI int slot_of(int c) { return (int)(((c < 16 ? SLOT_LO : SLOT_HI) >> (4 * (c & 15))) & 15); }

#define SB() __builtin_amdgcn_sched_barrier(0)
#define CFENCE() asm volatile("" ::: "memory")
template <int N> DI void wait_vm() { static_assert(N >= 0 && N < 64, "vmcnt"); asm volatile("s_waitcnt vmcnt(%0)" ::"n"(N) : "memory"); }
DI void bar() { asm volatile("s_waitcnt lgkmcnt(0)\n\ts_barrier" ::: "memory"); }
DI unsigned cvtpk(float lo, float hi) { f32x2 v = {lo, hi}; bf16x2 b = __builtin_convertvector(v, bf16x2); return __builtin_bit_cast(unsigned, b); }
DI bf16x8 pack8(const f32x16& x, int s) {
  u32x4 w = {cvtpk(x[8 * s], x[8 * s + 1]), cvtpk(x[8 * s + 2], x[8 * s + 3]), cvtpk(x[8 * s + 4], x[8 * s + 5]), cvtpk(x[8 * s + 6], x[8 * s + 7])};
  return __builtin_bit_cast(bf16x8, w);
}
DI bf16x4 trread(unsigned a) { return __builtin_amdgcn_ds_read_tr16_b64_v4bf16((LAS bf16x4*)(size_t)a); }
DI bf16x8 tr2(unsigned a0, unsigned a1) { bf16x4 lo = trread(a0), hi = trread(a1); return __builtin_shufflevector(lo, hi, 0, 1, 2, 3, 4, 5, 6, 7); }
DI float swap32max(float v) { auto r = __builtin_amdgcn_permlane32_swap(__float_as_uint(v), __float_as_uint(v), false, false); return fmaxf(__uint_as_float(r[0]), __uint_as_float(r[1])); }
DI float swap32sum(float v) { auto r = __builtin_amdgcn_permlane32_swap(__float_as_uint(v), __float_as_uint(v), false, false); return __uint_as_float(r[0]) + __uint_as_float(r[1]); }
DI int lane_id() { int l; asm volatile("v_mbcnt_lo_u32_b32 %0, -1, 0\n\tv_mbcnt_hi_u32_b32 %0, -1, %0" : "=v"(l)); return l; }
DI float max3(float a, float b, float c) { float r; asm volatile("v_max3_f32 %0, %1, %2, %3" : "=v"(r) : "v"(a), "v"(b), "v"(c)); return r; }
DI unsigned opaque(unsigned v) { asm volatile("" : "+v"(v)); return v; }
#define MFMA32(a, b, c) __builtin_amdgcn_mfma_f32_32x32x16_bf16((a), (b), (c), 0, 0, 0)
#define MFMA16(a, b, c) __builtin_amdgcn_mfma_f32_16x16x32_bf16((a), (b), (c), 0, 0, 0)
#define SGB(mask, n) __builtin_amdgcn_sched_group_barrier((mask), (n), 0)
template <int I, int N, class F> DI void static_for(F&& f) { if constexpr (I < N) { f(std::integral_constant<int, I>{}); static_for<I + 1, N>(f); } }
template <int I> using IC = std::integral_constant<int, I>;

template <bool NTL> DI void dma4(unsigned lds, const char* g, unsigned v0, unsigned v1, unsigned v2, unsigned v3) {
  unsigned keep;
  if constexpr (NTL)
    asm volatile("s_mov_b32 %0, m0\n\ts_mov_b32 m0, %1\n\ts_nop 3\n\tglobal_load_lds_dwordx4 %3, %2 nt\n\t"
                 "s_add_u32 m0, m0, 0x400\n\ts_nop 0\n\tglobal_load_lds_dwordx4 %4, %2 nt\n\t"
                 "s_add_u32 m0, m0, 0x400\n\ts_nop 0\n\tglobal_load_lds_dwordx4 %5, %2 nt\n\t"
                 "s_add_u32 m0, m0, 0x400\n\ts_nop 0\n\tglobal_load_lds_dwordx4 %6, %2 nt\n\ts_mov_b32 m0, %0"
                 : "=&s"(keep) : "s"(lds), "s"(g), "v"(v0), "v"(v1), "v"(v2), "v"(v3) : "memory", "scc");
  else
    asm volatile("s_mov_b32 %0, m0\n\ts_mov_b32 m0, %1\n\ts_nop 3\n\tglobal_load_lds_dwordx4 %3, %2\n\t"
                 "s_add_u32 m0, m0, 0x400\n\ts_nop 0\n\tglobal_load_lds_dwordx4 %4, %2\n\t"
                 "s_add_u32 m0, m0, 0x400\n\ts_nop 0\n\tglobal_load_lds_dwordx4 %5, %2\n\t"
                 "s_add_u32 m0, m0, 0x400\n\ts_nop 0\n\tglobal_load_lds_dwordx4 %6, %2\n\ts_mov_b32 m0, %0"
                 : "=&s"(keep) : "s"(lds), "s"(g), "v"(v0), "v"(v1), "v"(v2), "v"(v3) : "memory", "scc");
}

DI void dma3(unsigned lds, const char* g, unsigned v0, unsigned v1, unsigned v2) {
  unsigned keep;
  asm volatile("s_mov_b32 %0, m0\n\ts_mov_b32 m0, %1\n\ts_nop 3\n\tglobal_load_lds_dwordx4 %3, %2\n\t"
               "s_add_u32 m0, m0, 0x400\n\ts_nop 0\n\tglobal_load_lds_dwordx4 %4, %2\n\t"
               "s_add_u32 m0, m0, 0x400\n\ts_nop 0\n\tglobal_load_lds_dwordx4 %5, %2\n\ts_mov_b32 m0, %0"
               : "=&s"(keep) : "s"(lds), "s"(g), "v"(v0), "v"(v1), "v"(v2) : "memory", "scc");
}

#define STAMP(i) do {} while (0)
#define STAMPL(i) do {} while (0)
#define CSTAMP(i) do {} while (0)

__global__ __launch_bounds__(512, 2) void fused_kernel(const u16* __restrict__ x, const u16* __restrict__ kc, const u16* __restrict__ vc,
                                                      const u16* __restrict__ wq, const u16* __restrict__ wo, u16* __restrict__ out, char* ws) {
  extern __shared__ __attribute__((aligned(16))) char smem[];
  const char* pc0;
  asm volatile("s_getpc_b64 %0" : "=s"(pc0));
  const int tid = threadIdx.x;
  const int wid = __builtin_amdgcn_readfirstlane(tid >> 6);
  const int bid = blockIdx.x, xcd = bid & 7, idx = bid >> 3;
  const int h = xcd * 4 + (idx & 3), b = idx >> 2;
  const int rb = 8 * (xcd >> 2) + (idx >> 2), cb = 4 * (xcd & 3) + (idx & 3);
  const int kr = (wid + (idx >> 2)) & 7;
  const int rot = (b + 2 * (idx & 3)) & 7;
  const unsigned sbase = (unsigned)(size_t)(LAS char*)smem;
  volatile LAS unsigned* ctl = (volatile LAS unsigned*)(size_t)(sbase + CTRL);
  u32x4 wf[32];
  u64 tag;
  const __amdgpu_buffer_rsrc_t ors = __builtin_amdgcn_make_buffer_rsrc(ws + WS_O, 0, (int)(O_BYTES + WT_BYTES), 0x00020000);
  const unsigned wt_off = (unsigned)O_BYTES + (unsigned)(cb * 128 + kr * 16) * 2048;
  unsigned done_seen = 0;
  auto wait_done = [&](unsigned need) AI {
    if (done_seen < need) {
#pragma unroll 1
      for (unsigned sp = 0; sp < SPIN_CAP; ++sp) {
        const u32x4 d = *(volatile LAS u32x4*)(size_t)(sbase + CTRL + 4 * W_DONE);
        done_seen = __builtin_amdgcn_readfirstlane(min(d.x, min(d.y, d.z)));
        if (done_seen >= need) break;
        __builtin_amdgcn_s_sleep(1);
      }
    }
    CFENCE();
  };
  auto wait_word = [&](int w) AI {
#pragma unroll 1
    for (unsigned sp = 0; sp < SPIN_CAP; ++sp) { if (__builtin_amdgcn_readfirstlane(ctl[w]) != 0u) break; __builtin_amdgcn_s_sleep(1); }
    CFENCE();
  };
  auto wait_landed = [&](unsigned n) AI {
#pragma unroll 1
    for (unsigned sp = 0; sp < SPIN_CAP; ++sp) { if ((__builtin_amdgcn_readfirstlane(ctl[W_FULL]) & 255u) >= n) break; __builtin_amdgcn_s_sleep(2); }
    CFENCE();
  };
  auto get_tag = [&]() AI { wait_word(W_TAG + 2); tag = (u64)ctl[W_TAG] | ((u64)ctl[W_TAG + 1] << 32); };

  if (__builtin_expect(wid >= 4, 1)) {
    const int lane = tid & 63, q = wid - 4;
    if (q == 3) {
      const unsigned co = min((unsigned)(idx * 8 + (lane & 7)) * 128u, (unsigned)CODE_BYTES);
      unsigned keep;
      asm volatile("s_mov_b32 %0, m0\n\ts_mov_b32 m0, %1\n\ts_nop 3\n\tglobal_load_lds_dword %3, %2\n\ts_mov_b32 m0, %0"
                   : "=&s"(keep) : "s"(sbase + SCR2 + 2048 + 512), "s"(pc0), "v"(co) : "memory");
    }
    const unsigned tstart = (unsigned)__builtin_amdgcn_s_memrealtime();
    const int r7 = lane >> 3, sl = lane & 7;
    const unsigned c0 = (sl ^ (((r7 & 2) << 1) | (r7 >> 2))) * 16, c1 = c0 ^ 32;
    const unsigned kv0 = r7 * 256 + c0, kv2 = (8 + r7) * 256 + c1, w0 = r7 * 8192 + c0, w2 = (8 + r7) * 8192 + c1;
    const char* kb = (const char*)kc + (size_t)(b * NH + h) * SK * DH * 2 + q * 4096;
    const char* vb = (const char*)vc + (size_t)(b * NH + h) * SK * DH * 2 + q * 4096;
    const char* wb = (const char*)wq + h * DH * 2 + q * 16 * 8192;
    const unsigned ldsq = sbase + q * 4096;
    volatile LAS unsigned char* fullb = (volatile LAS unsigned char*)(size_t)(sbase + CTRL + 4 * W_FULL);
    const char* xb = (const char*)x + (size_t)(b * SQ + q * 24) * (DM * 2);
    const unsigned xc = c0 ^ ((q & 1) * 32), x0 = r7 * 1024 + xc, x1 = (8 + r7) * 1024 + (xc ^ 32), x2 = x0 + 16 * 1024;
    auto ldx = [&](int j) AI { dma3(sbase + XREG + (j % 5) * XAREA + q * 3072, xb + ((j + rot) & 7) * 128, x0, x1, x2); };
    static_for<0, NT>([&](auto N) AI {
      constexpr int n = N.value, c = tile_at(n);
      if constexpr (need_tab[c] > 0) wait_done(need_tab[c]);
      if constexpr (PACE_T > 0 && n >= 12) {
#pragma unroll 1
        while ((unsigned)__builtin_amdgcn_s_memrealtime() - tstart < (unsigned)(PACE_A + (n - 10) * PACE_T)) __builtin_amdgcn_s_sleep(2);
      }
      const unsigned lds = ldsq + slot_tab[c] * SLOTB;
      if constexpr (c < 8) {
        dma4<false>(lds, wb + (size_t)((c + rot) & 7) * 64 * 8192, w0, w0 + 128, w2, w2 + 128);
        if constexpr (c == 0) ldx(0);
        if constexpr (c < 7) ldx(c + 1);
        if constexpr (c == 0) bar();
      } else {
        constexpr bool isk = (c == 8) || ((c & 1) && c != 23);
        constexpr int ti = c == 8 ? 0 : (c == 23 ? 7 : ((c & 1) ? (c - 7) >> 1 : (c - 10) >> 1));
        if constexpr (isk) dma4<K_NT != 0>(lds, kb + ti * SLOTB, kv0, kv0 + 128, kv2, kv2 + 128);
        else dma4<V_NT != 0>(lds, vb + ti * SLOTB, kv0, kv0 + 128, kv2, kv2 + 128);
      }
      if constexpr (n == 9) { wait_vm<inflight(9, n)>(); fullb[q] = 9; CFENCE(); STAMP(9); }
      if constexpr (pubto(n) > (n ? pubto(n - 1) : 0)) { wait_vm<inflight(pubto(n), n)>(); fullb[q] = (unsigned char)pubto(n); CFENCE(); STAMP(pubto(n)); }
    });
    static_for<0, WIN>([&](auto I) AI {
      constexpr int i = I.value;
      wait_vm<4 * (WIN - 1 - i)>(); fullb[q] = (unsigned char)(NT - WIN + i + 1); CFENCE(); STAMP(1 + NT - WIN + i);
    });
    wait_word(W_WT);
#pragma unroll
    for (int e = 0; e < WF_EARLY; ++e) wf[e] = __builtin_amdgcn_raw_buffer_load_b128(ors, wt_off + lane * 16, e * 1024, 16);
    SB();
    wait_done(NT);
#pragma unroll
    for (int e = WF_EARLY; e < 32; ++e) wf[e] = __builtin_amdgcn_raw_buffer_load_b128(ors, wt_off + lane * 16, e * 1024, 16);
    get_tag();
    STAMP(33);
  } else if (wid == 3) {
    const int lane = tid & 63;
    const int ks = bid >> 1, half = bid & 1;
    {
      const char* src = (const char*)wo + (size_t)(ks * 32 + (lane >> 3)) * 1024 + half * 512 + (lane & 7) * 16;
      u32x4 raw[16];
      if (lane < 16) ctl[lane] = 0u;
      bar();
      if (AUX_LD > 0) wait_landed(AUX_LD);
      tag = __hip_atomic_load((gu64*)(ws + WS_EPOCH), RLX_AGENT);
#pragma unroll
      for (int j = 0; j < 4; ++j)
#pragma unroll
        for (int a = 0; a < 4; ++a) raw[4 * j + a] = __builtin_nontemporal_load((const u32x4*)(src + a * 8192 + j * 128));
      SB();
      const int g4 = lane >> 4, qq = (lane & 15) >> 2, p = lane & 3;
      const unsigned sc = sbase + SCR2, ta = sc + 128 * (4 * (g4 & 1) + qq) + 32 * (g4 >> 1) + 8 * p;
      const unsigned dst = (unsigned)O_BYTES + (unsigned)(half * 8 * 128 + ks) * 2048 + (lane >> 5) * 1024 + (lane & 31) * 16;
#pragma unroll
      for (int ps = 0; ps < 8; ++ps) {
        *(LAS u32x4*)(size_t)(sc + lane * 16) = raw[2 * ps];
        *(LAS u32x4*)(size_t)(sc + 1024 + lane * 16) = raw[2 * ps + 1];
        CFENCE();
#pragma unroll
        for (int m = 0; m < 2; ++m) raw[2 * ps + m] = __builtin_bit_cast(u32x4, tr2(ta + m * 64, ta + m * 64 + 1024));
        CFENCE();
      }
      if (AUX_ST > 0) wait_landed(AUX_ST);
#pragma unroll
      for (int e = 0; e < 16; ++e) __builtin_amdgcn_raw_buffer_store_b128(raw[e], ors, dst + ((e >> 1) & 1) * 512, (2 * (e >> 2) + (e & 1)) * 128 * 2048, 16);
      ctl[W_TAG] = (unsigned)tag; ctl[W_TAG + 1] = (unsigned)(tag >> 32);
      CFENCE();
      ctl[W_TAG + 2] = 1u;
      wait_vm<0>();
      if (lane == 0) __hip_atomic_store((gu64*)(ws + WS_FLAG2) + half * 128 + ks, tag + TAG_STEP, RLX_AGENT);
    }
    STAMP(1);
    wait_done(AUX_AT);
    {
      const __amdgpu_buffer_rsrc_t frs = __builtin_amdgcn_make_buffer_rsrc(ws + WS_FLAG2 + (cb >> 3) * 1024, 0, 1024, 0x00020000);
      const unsigned tl = (unsigned)(tag + TAG_STEP), th = (unsigned)((tag + TAG_STEP) >> 32);
#pragma unroll 1
      for (unsigned sp = 0; sp < SPIN_CAP; ++sp) {
        const u32x4 v = __builtin_amdgcn_raw_buffer_load_b128(frs, lane * 16, 0, 16);
        if (__all(v.x == tl && v.y == th && v.z == tl && v.w == th)) break;
        __builtin_amdgcn_s_sleep(32);
      }
      CFENCE();
      ctl[W_WT] = 1u;
      CFENCE();
    }
    STAMP(2);
    wait_landed(NT - WIN);
#pragma unroll
    for (int e = 0; e < 32; ++e) wf[e] = __builtin_amdgcn_raw_buffer_load_b128(ors, wt_off + lane * 16, e * 1024, 16);
    STAMP(33);
    wait_done(NT);
  } else {
    int lane = tid & 63;
    int r = lane & 31, hh = lane >> 5, blk = (lane >> 4) & 1, qq = (lane & 15) >> 2, p = lane & 3;
    const int ps = ((p & 1) << 1) | (p >> 1);
    bar();
    unsigned fl;
    const unsigned cta = opaque(sbase + CTRL), dna = opaque(sbase + CTRL + 4 * (W_DONE + wid));
    unsigned dnv = 0;
    auto lacking = [](unsigned v, int n) AI { return (((unsigned)__builtin_amdgcn_readfirstlane(v) - (unsigned)(n + 1) * 0x01010101u) & 0x80808080u) != 0u; };
    auto peek = [&]() AI { fl = *(volatile LAS unsigned*)(size_t)(cta + 4 * W_FULL); };
    auto wait_full = [&](int n) AI {
      if (__builtin_expect(lacking(fl, n), 0)) {
#pragma unroll 1
        for (unsigned sp = 0; sp < SPIN_CAP; ++sp) { if (!lacking(*(volatile LAS unsigned*)(size_t)(cta + 4 * W_FULL), n)) break; }
      }
      CFENCE();
    };
    auto release = [&]() AI { CFENCE(); dnv += 1; asm volatile("" : "+v"(dnv)); *(volatile LAS unsigned*)(size_t)dna = dnv; CFENCE(); };
    const unsigned wa = sbase + 2048 * hh + 128 * qq + 16 * ((2 * blk + (ps >> 1)) ^ (((qq & 2) << 1) | (2 * hh))) + 8 * (ps & 1);
    const unsigned va = sbase + 128 * (4 * hh + qq) + 16 * ((2 * blk + (p >> 1)) ^ (((qq & 2) << 1) | hh)) + 8 * (p & 1);
    const unsigned fr = 128 * (r & 7) + 16 * (hh ^ (((r & 2) << 1) | ((r >> 2) & 3)));
    const unsigned ka = sbase + 2048 * (r >> 3) + fr;
    const unsigned xa = sbase + XREG + 1024 * (4 * wid + (r >> 3)) + fr;
    f32x16 acc[4];
    bf16x8 FR[8];
    unsigned ab[4], kb4[4];
    auto setab = [&](unsigned (&d)[4], unsigned a, unsigned x1, unsigned x2) AI { d[0] = opaque(a); d[1] = opaque(a ^ x1); d[2] = opaque(a ^ x2); d[3] = opaque(a ^ x1 ^ x2); };
    auto seta = [&](unsigned (&d)[4], unsigned a, int j) AI { d[j] = opaque(j == 0 ? a : d[0] ^ (32 * j)); };
    auto rdw = [&](int i, int so) AI { const int ks = i >> 2, c = i & 3, o = so + 4096 * ks + 1024 * (c >> 1); FR[i & 7] = tr2(ab[2 * (c & 1)] + o, ab[2 * (c & 1) + 1] + o + 512); };
    auto rdv = [&](int i) AI { const int ks = i >> 2, c = i & 3, o = 4096 * ks + 1024 * (c >> 1); FR[i & 7] = tr2(ab[2 * (c & 1)] + o, ab[2 * (c & 1) + 1] + o + 2048); };
    auto rdk = [&](int i) AI { FR[i & 7] = *(LAS bf16x8*)(size_t)(kb4[i & 3] + 1024 * ((i >> 2) & 1) + 8192 * (i >> 3)); };
    auto ready = [&](int c) AI { wait_full(c); STAMPL(1 + c); SB(); };
    {
      bf16x8 XF[4];
      unsigned xb4[4] = {opaque(xa), opaque(xa ^ 32), opaque(xa ^ 64), opaque(xa ^ 96)};
      auto rdx = [&](int tile, int ks) AI { XF[ks] = *(LAS bf16x8*)(size_t)(xb4[ks] + (tile % 5) * XAREA); };
      setab(ab, wa, 16, 64);
      setab(kb4, ka + slot_tab[8] * SLOTB, 32, 64);
      peek(); wait_full(0); STAMP(1); CSTAMP(29);
      rdx(0, 0); rdx(0, 1);
#pragma unroll
      for (int i = 0; i < 6; ++i) { rdw(i ^ 1, 0); SB(); }
      SB();
      static_for<0, 8>([&](auto TL) AI {
        constexpr int tile = TL.value;
        static_for<0, 16>([&](auto I) AI {
          constexpr int i = I.value;
          if constexpr (i == 10) { wait_full(order_of(tile + 1)); STAMPL(2 + tile); SB(); }
          acc[i & 3] = MFMA32(FR[i & 7], XF[i >> 2], (tile == 0 && i < 4) ? f32x16{} : acc[i & 3]);
          SB();
          if constexpr ((i & 1) == 0) {
            if constexpr (i < 10) { rdw(i + 7, (slot_tab[tile] & 3) * SLOTB); SB(); rdw(i + 6, (slot_tab[tile] & 3) * SLOTB); }
            else if constexpr (tile < 7) { rdw(i - 9, (slot_tab[tile + 1] & 3) * SLOTB); SB(); rdw(i - 10, (slot_tab[tile + 1] & 3) * SLOTB); }
            else { rdk(i - 9); SB(); rdk(i - 10); }
          }
          if constexpr (i == 1) rdx(tile, 2);
          if constexpr (i == 5) rdx(tile, 3);
          if constexpr (i == 7) peek();
          if constexpr (i == 9) release();
          if constexpr (i == 11 && tile < 7) rdx(tile + 1, 0);
          if constexpr (i == 13 && tile < 7) rdx(tile + 1, 1);
          SB();
        });
      });
    }
    STAMP(9); CSTAMP(30);
    const float CS = 0.08838834764831845f * 1.4426950408889634f;
    bf16x8 qf[8];
#pragma unroll
    for (int c = 0; c < 4; ++c) { acc[c] *= CS; qf[2 * c] = pack8(acc[c], 0); qf[2 * c + 1] = pack8(acc[c], 1); }
#pragma unroll
    for (int c = 0; c < 4; ++c) acc[c] = f32x16{};
    float l = 0.f, alpha = 1.f, rs0 = 0.f, rs1 = 0.f, mxa, mxb;
    bool pend = false;
    f32x16 s0, s1, negm;
    float e0[16], e1[16];
    unsigned pw[16];
    auto pin_acc = [&]() AI { asm volatile("" : "+v"(acc[0]), "+v"(acc[1]), "+v"(acc[2]), "+v"(acc[3])); };
#define PIN(v) asm volatile("" : "+v"(v))
    auto ex0 = [&](int i) AI { e0[i] = __builtin_amdgcn_exp2f(s0[i]); PIN(e0[i]); };
    auto ex1 = [&](int i) AI { e1[i] = __builtin_amdgcn_exp2f(s1[i]); PIN(e1[i]); };
    auto ad0 = [&](int i) AI { rs0 += e0[i]; PIN(rs0); rs1 += e0[i + 1]; PIN(rs1); };
    auto ad1 = [&](int i) AI { rs0 += e1[i]; PIN(rs0); rs1 += e1[i + 1]; PIN(rs1); };
    auto pk0 = [&](int w) AI { pw[w] = cvtpk(e0[2 * w], e0[2 * w + 1]); PIN(pw[w]); };
    auto pk1 = [&](int w) AI { pw[8 + w] = cvtpk(e1[2 * w], e1[2 * w + 1]); PIN(pw[8 + w]); };
    auto pfrag = [&](int k) AI { u32x4 v = {pw[4 * k], pw[4 * k + 1], pw[4 * k + 2], pw[4 * k + 3]}; return __builtin_bit_cast(bf16x8, v); };
    auto mx0 = [&](int i) AI { float& m = (i & 1) ? mxb : mxa; m = i < 2 ? fmaxf(s0[2 * i], s0[2 * i + 1]) : fmaxf(fmaxf(m, s0[2 * i]), s0[2 * i + 1]); PIN(m); };
    auto mx1 = [&](int i) AI { float& m = (i & 1) ? mxb : mxa; m = fmaxf(fmaxf(m, s1[2 * i]), s1[2 * i + 1]); PIN(m); };
    auto shift_ref = [&](float d) AI {
#pragma unroll
      for (int e = 0; e < 16; ++e) { s0[e] -= d; s1[e] -= d; negm[e] -= d; }
    };
    auto decide = [&]() AI {
      const float mx = fmaxf(mxa, mxb);
      if (__builtin_expect(__any(mx > 8.f), 0)) {
        const float d = fmaxf(swap32max(mx), 0.f);
        shift_ref(d);
        alpha = __builtin_amdgcn_exp2f(-d);
        l = (l + rs0 + rs1) * alpha; rs0 = 0.f; rs1 = 0.f;
        pend = true;
      }
      SB();
    };
    auto rescale = [&]() AI {
      if (__builtin_expect(pend, 0)) {
#pragma unroll
        for (int cc = 0; cc < 4; ++cc) acc[cc] *= alpha;
        pend = false;
      }
      SB();
    };
    negm = f32x16{};
    static_for<0, 16>([&](auto I) AI {
      constexpr int i = I.value;
      if constexpr (i == 10) { ready(9); setab(kb4, ka + slot_tab[9] * SLOTB, 32, 64); SB(); }
      if constexpr (i < 8) s0 = MFMA32(FR[i & 7], qf[i & 7], (i == 0 ? negm : s0)); else s1 = MFMA32(FR[i & 7], qf[i & 7], (i == 8 ? negm : s1));
        if constexpr (i < 8) PIN(s0); else PIN(s1);
      SB();
      if constexpr ((i & 1) == 0) { rdk((i + 7) & 15); SB(); rdk((i + 6) & 15); }
      if constexpr (i == 7) peek();
      if constexpr (i == 9) release();
      if constexpr (i >= 11 && i < 15) { mx0(2 * (i - 11)); mx0(2 * (i - 11) + 1); }
      SB();
    });
#pragma unroll
    for (int i = 0; i < 8; ++i) mx1(i);
    shift_ref(swap32max(fmaxf(mxa, mxb)));
#pragma unroll
    for (int i = 0; i < 16; ++i) ex0(i);
#pragma unroll
    for (int i = 0; i < 8; ++i) { ad0(2 * i); pk0(i); }
#pragma unroll
    for (int i = 0; i < 4; ++i) ex1(i);
    SB();
    u64 seq = pack_slots(10);
#pragma unroll 1
    for (int c = 9; c < 23; c += 2) {
      rescale();
      static_for<0, 16>([&](auto I) AI {
        constexpr int i = I.value;
        if constexpr (i == 10) ready(c + 1);
        if constexpr (i < 8) s0 = MFMA32(FR[i & 7], qf[i & 7], (i == 0 ? negm : s0)); else s1 = MFMA32(FR[i & 7], qf[i & 7], (i == 8 ? negm : s1));
        if constexpr (i < 8) PIN(s0); else PIN(s1);
        SB();
        if constexpr ((i & 1) == 0) { if constexpr (i < 10) { rdk(i + 7); SB(); rdk(i + 6); } else { rdv(i - 9); SB(); rdv(i - 10); } }
        if constexpr (i == 0) { ex1(4); pk1(0); seta(ab, va + ((unsigned)seq & 15u) * SLOTB, 0); }
        if constexpr (i == 1) { ex1(5); ex1(6); ad1(0); }
        if constexpr (i == 2) { ex1(7); pk1(1); seta(ab, 0, 1); }
        if constexpr (i == 3) { ex1(8); ex1(9); ad1(2); }
        if constexpr (i == 4) { ex1(10); pk1(2); seta(ab, 0, 2); }
        if constexpr (i == 5) { ex1(11); ex1(12); ad1(4); }
        if constexpr (i == 6) { ex1(13); pk1(3); seta(ab, 0, 3); }
        if constexpr (i == 7) { ex1(14); ex1(15); peek(); }
        if constexpr (i == 8) { ad1(6); pk1(4); }
        if constexpr (i == 9) { release(); }
        if constexpr (i == 10) { pk1(5); }
        if constexpr (i == 11) { mx0(0); mx0(1); ad1(8); }
        if constexpr (i == 12) { pk1(6); }
        if constexpr (i == 13) { mx0(2); mx0(3); ad1(10); mx0(4); mx0(5); }
        if constexpr (i == 14) { pk1(7); }
        if constexpr (i == 15) { mx0(6); mx0(7); ad1(12); ad1(14); }
        SB();
      });
      static_for<0, 16>([&](auto I) AI {
        constexpr int i = I.value;
        if constexpr (i == 10) ready(c == 21 ? 21 : c + 2);
        acc[i & 3] = MFMA32(FR[i & 7], pfrag(i >> 2), acc[i & 3]);
        PIN(acc[i & 3]);
        SB();
        if constexpr ((i & 1) == 0) { if constexpr (i < 10) { rdv(i + 7); SB(); rdv(i + 6); } else { rdk(i - 9); SB(); rdk(i - 10); } }
        if constexpr (i == 0) { seta(kb4, ka + ((unsigned)(seq >> 4) & 15u) * SLOTB, 0); seq >>= 8; }
        if constexpr (i == 1) { mx1(0); mx1(1); mx1(2); mx1(3); mx1(4); mx1(5); }
        if constexpr (i == 2) { mx1(6); mx1(7); }
        if constexpr (i == 3) { decide(); ex0(0); ex0(1); }
        if constexpr (i == 4) { seta(kb4, 0, 1); }
        if constexpr (i == 5) { ex0(2); ex0(3); ad0(0); }
        if constexpr (i == 6) { ex0(4); seta(kb4, 0, 2); }
        if constexpr (i == 7) { ex0(5); ex0(6); ad0(2); peek(); }
        if constexpr (i == 8) { ex0(7); seta(kb4, 0, 3); }
        if constexpr (i == 9) { release(); ex0(8); }
        if constexpr (i == 10) { ex0(9); ad0(4); pk0(0); }
        if constexpr (i == 11) { ex0(10); ex0(11); ad0(6); pk0(1); pk0(2); }
        if constexpr (i == 12) { ex0(12); ex0(13); pk0(3); pk0(4); }
        if constexpr (i == 13) { ex0(14); ex0(15); ad0(8); pk0(5); }
        if constexpr (i == 14) { ex1(0); ex1(1); ad0(10); pk0(6); }
        if constexpr (i == 15) { ex1(2); ex1(3); ad0(12); ad0(14); pk0(7); }
        SB();
      });
    }
    rescale();
#pragma unroll
    for (int i = 4; i < 16; ++i) ex1(i);
#pragma unroll
    for (int i = 0; i < 8; ++i) { ad1(2 * i); pk1(i); }
    const float linv = 1.f / swap32sum(l + rs0 + rs1);
    get_tag();
    peek();
    wait_full(23);
    STAMP(24); CSTAMP(31);
    setab(ab, va + slot_tab[23] * SLOTB, 32, 64);
#pragma unroll
    for (int i = 0; i < 6; ++i) rdv(i);
    SB();
    static_for<0, 16>([&](auto I) AI {
      constexpr int i = I.value;
      acc[i & 3] = MFMA32(FR[i & 7], pfrag(i >> 2), acc[i & 3]);
      SB();
      if constexpr ((i & 1) == 0 && i < 10) { rdv(i + 7); SB(); rdv(i + 6); }
      SB();
    });
    STAMP(25);
    lane = lane_id(); r = lane & 31; hh = lane >> 5;
    const int u = 2 * wid + (r >> 4);
    const unsigned ob = (((b * 6 + u) * 128 + h * 4) * 64 + hh * 16 + (r & 15)) * 16;
#pragma unroll
    for (int c = 0; c < 4; ++c) {
#pragma unroll
      for (int e = 0; e < 16; ++e) acc[c][e] *= linv;
      __builtin_amdgcn_raw_buffer_store_b128(__builtin_bit_cast(u32x4, pack8(acc[c], 0)), ors, ob + c * 1024, 0, 16);
      __builtin_amdgcn_raw_buffer_store_b128(__builtin_bit_cast(u32x4, pack8(acc[c], 1)), ors, ob + c * 1024 + 512, 0, 16);
    }
    SB();
    release();
    STAMP(26);
    wait_vm<0>();
    STAMP(27);
    {
      unsigned old = 0;
      if (lane == 0) old = __hip_atomic_fetch_add((LAS unsigned*)(size_t)(sbase + CTRL + 4 * W_OCNT), 1u, RLX_WG);
      old = __builtin_amdgcn_readfirstlane(old);
      if (old == 2u && lane == 0) __hip_atomic_store((gu64*)(ws + WS_FLAGS) + (size_t)(b * NH + h) * FLAG_STRIDE, tag + TAG_STEP, RLX_AGENT);
      SB();
    }
    wait_word(W_WT);
#pragma unroll
    for (int e = 0; e < 32; ++e) wf[e] = __builtin_amdgcn_raw_buffer_load_b128(ors, wt_off + lane * 16, e * 1024, 16);
    STAMP(28);
  }
  tag += TAG_STEP;
  const int lane = lane_id(), g4 = lane >> 4;
  {
    gu64* fp = (gu64*)(ws + WS_FLAGS) + (size_t)((rb >> 1) * NH + kr * 4 + (lane & 3)) * FLAG_STRIDE;
#pragma unroll 1
    for (unsigned sp = 0; sp < SPIN_CAP; ++sp) {
      const u64 f = __hip_atomic_load(fp, RLX_AGENT);
      if (__all(f == tag)) break;
      __builtin_amdgcn_s_sleep(1);
    }
    CFENCE();
  }
  STAMP(34);
  f32x4 oa[2][3];
#pragma unroll
  for (int nb = 0; nb < 2; ++nb)
#pragma unroll
    for (int u = 0; u < 3; ++u) oa[nb][u] = f32x4{};
  {
    const unsigned obase = ((rb * 3) * 128 + kr * 16) * 1024 + lane * 16;
    u32x4 of[2][3][4];
    auto ldo = [&](int hd) AI {
#pragma unroll
      for (int ks = 0; ks < 4; ++ks)
#pragma unroll
        for (int u = 0; u < 3; ++u) of[hd & 1][u][ks] = __builtin_amdgcn_raw_buffer_load_b128(ors, obase + (hd * 4 + ks) * 1024, u * 131072, 16);
    };
    ldo(0); ldo(1);
    SB();
#pragma unroll
    for (int hd = 0; hd < 4; ++hd) {
#pragma unroll
      for (int ks = 0; ks < 4; ++ks)
#pragma unroll
        for (int u = 0; u < 3; ++u)
#pragma unroll
          for (int nb = 0; nb < 2; ++nb) oa[nb][u] = MFMA16(__builtin_bit_cast(bf16x8, wf[(hd * 4 + ks) * 2 + nb]), __builtin_bit_cast(bf16x8, of[hd & 1][u][ks]), oa[nb][u]);
      SB();
      if (hd + 2 < 4) ldo(hd + 2);
      SB();
    }
  }
  STAMP(35);
  bar();
  LAS f32x4* red = (LAS f32x4*)(size_t)sbase;
#pragma unroll
  for (int nb = 0; nb < 2; ++nb)
#pragma unroll
    for (int u = 0; u < 3; ++u) red[(wid * 6 + nb * 3 + u) * 64 + lane] = oa[nb][u];
  bar();
  if (wid < 6) {
    f32x4 s = red[wid * 64 + lane];
#pragma unroll
    for (int w = 1; w < 8; ++w) s += red[(w * 6 + wid) * 64 + lane];
    const int nb = wid / 3, u = wid % 3;
    const int qrow = rb * 48 + u * 16 + (lane & 15), n = cb * 32 + nb * 16 + g4 * 4;
    u32x2 o = {cvtpk(s[0], s[1]), cvtpk(s[2], s[3])};
    *(u32x2*)(out + (size_t)qrow * DM + n) = o;
  } else if (wid == 7 && bid == 0) {
    bool ok = false;
#pragma unroll 1
    for (unsigned sp = 0; sp < SPIN_CAP && !ok; ++sp) {
      bool all = true;
#pragma unroll
      for (int i = 0; i < 4; ++i) all &= __hip_atomic_load((gu64*)(ws + WS_FLAGS) + (size_t)(i * 64 + lane) * FLAG_STRIDE, RLX_AGENT) == tag;
      ok = __all(all);
    }
    if (lane == 0) __hip_atomic_store((gu64*)(ws + WS_EPOCH), tag, RLX_AGENT);
  }
}

extern "C" void kernel_launch(void* const* d_in, const int* in_sizes, int n_in, void* d_out, int out_size, void* d_ws, size_t ws_size,
                              hipStream_t stream) {
  (void)hipFuncSetAttribute((const void*)fused_kernel, hipFuncAttributeMaxDynamicSharedMemorySize, LDS_BYTES);
  fused_kernel<<<256, 512, LDS_BYTES, stream>>>((const u16*)d_in[0], (const u16*)d_in[1], (const u16*)d_in[2], (const u16*)d_in[3],
                                                (const u16*)d_in[4], (u16*)d_out, (char*)d_ws);
}
```

```cpp
#include <hip/hip_runtime.h>
#include <hip/hip_bf16.h>
#include <utility>

#define LAS __attribute__((address_space(3)))
#define GAS __attribute__((address_space(1)))
#define DI __device__ __forceinline__
#define AI __attribute__((always_inline))
typedef __bf16 bf16x8 __attribute__((ext_vector_type(8)));
typedef __bf16 bf16x4 __attribute__((ext_vector_type(4)));
typedef __bf16 bf16x2 __attribute__((ext_vector_type(2)));
typedef float f32x16 __attribute__((ext_vector_type(16)));
typedef float f32x4 __attribute__((ext_vector_type(4)));
typedef float f32x2 __attribute__((ext_vector_type(2)));
typedef unsigned u32x4 __attribute__((ext_vector_type(4)));
typedef unsigned u32x2 __attribute__((ext_vector_type(2)));
typedef short s16x2 __attribute__((ext_vector_type(2)));
typedef unsigned short u16;
typedef unsigned long long u64;
typedef GAS u64 gu64;
#define RLX_AGENT __ATOMIC_RELAXED, __HIP_MEMORY_SCOPE_AGENT
#define RLX_WG __ATOMIC_RELAXED, __HIP_MEMORY_SCOPE_WORKGROUP

#define K_NT 1
#define V_NT 1
#define WIN 3
#define WINQ 1
#define AUX_LD 3
#define PACE_A 600
#define PACE_T 54
#define FREE_AT (NT - WIN)
#define AUX_ST 0
#define WF_EARLY 0
#define AUX_AT 10

constexpr int NB = 8, SQ = 96, DM = 512, NH = 32, SK = 512, DH = 128;
constexpr int NSLOT = 9, SLOTB = 16384, NT = 24;
constexpr int XREG = 6 * SLOTB, XAREA = 12288;
constexpr int CTRL = XREG + 5 * XAREA;
constexpr int SCR2 = CTRL + 1024;
constexpr int LDS_BYTES = 163840;
constexpr int W_FULL = 0, W_DONE = 4, W_OCNT = 8, W_TAG = 12, W_WT = 15;
constexpr size_t WS_EPOCH = 0, WS_FLAGS = 4096, WS_FLAG2 = 40960, WS_O = 65536, O_BYTES = (size_t)NB * 6 * 128 * 1024, WS_WT = WS_O + O_BYTES, WT_BYTES = (size_t)NH * DH * DM * 2;
constexpr int CODE_BYTES = 26112;
constexpr int FLAG_STRIDE = 16;
constexpr unsigned SPIN_CAP = 1u << 20;
constexpr u64 TAG_STEP = 0x9E3779B97F4A7C15ull;

constexpr int slot_tab[NT] = {0, 1, 2, 3, 0, 1, 2, 3, 4, 5, 0, 1, 2, 6, 3, 7, 8, 4, 5, 0, 1, 2, 6, 3};
constexpr int need_tab[NT] = {0, 0, 0, 0, 1, 2, 3, 4, 0, 0, 5, 6, 7, 0, 8, 0, 0, 9, 10, 11, 12, 13, 14, 15};
constexpr int tile_at(int n) { return n < 4 ? n : (n < 6 ? n + 4 : (n < 10 ? n - 2 : n)); }
constexpr int order_of(int c) { return c < 4 ? c : (c < 8 ? c + 2 : (c < 10 ? c - 4 : c)); }
constexpr int nops(int n) { return n == 0 ? 10 : (tile_at(n) < 7 ? 7 : (n < 10 ? 4 : 8)); }
constexpr int pub_tab[13] = {1, 1, 2, 3, 3, 4, 4, 7, 8, 10, 10, 10, 10};
constexpr int pubto(int n) { return n < 10 ? pub_tab[n] : (n + 1 - WIN > 10 ? n + 1 - WIN : 10); }
constexpr int inflight(int p, int n) { int s = (p == 1 ? 3 : 0); for (int i = p; i <= n; ++i) s += nops(i); return s; }
constexpr u64 pack_slots(int lo) { u64 v = 0; for (int i = 0; i < 16 && lo + i < NT; ++i) v |= (u64)slot_tab[lo + i] << (4 * i); return v; }
constexpr u64 SLOT_LO = pack_slots(0), SLOT_HI = pack_slots(16);
DI int slot_of(int c) { return (int)(((c < 16 ? SLOT_LO : SLOT_HI) >> (4 * (c & 15))) & 15); }

#define SB() __builtin_amdgcn_sched_barrier(0)
#define CFENCE() asm volatile("" ::: "memory")
template <int N> DI void wait_vm() { static_assert(N >= 0 && N < 64, "vmcnt"); asm volatile("s_waitcnt vmcnt(%0)" ::"n"(N) : "memory"); }
DI void bar() { asm volatile("s_waitcnt lgkmcnt(0)\n\ts_barrier" ::: "memory"); }
DI unsigned cvtpk(float lo, float hi) { f32x2 v = {lo, hi}; bf16x2 b = __builtin_convertvector(v, bf16x2); return __builtin_bit_cast(unsigned, b); }
DI bf16x8 pack8(const f32x16& x, int s) {
  u32x4 w = {cvtpk(x[8 * s], x[8 * s + 1]), cvtpk(x[8 * s + 2], x[8 * s + 3]), cvtpk(x[8 * s + 4], x[8 * s + 5]), cvtpk(x[8 * s + 6], x[8 * s + 7])};
  return __builtin_bit_cast(bf16x8, w);
}
DI bf16x4 trread(unsigned a) { return __builtin_amdgcn_ds_read_tr16_b64_v4bf16((LAS bf16x4*)(size_t)a); }
DI bf16x8 tr2(unsigned a0, unsigned a1) { bf16x4 lo = trread(a0), hi = trread(a1); return __builtin_shufflevector(lo, hi, 0, 1, 2, 3, 4, 5, 6, 7); }
DI float swap32max(float v) { auto r = __builtin_amdgcn_permlane32_swap(__float_as_uint(v), __float_as_uint(v), false, false); return fmaxf(__uint_as_float(r[0]), __uint_as_float(r[1])); }
DI float swap32sum(float v) { auto r = __builtin_amdgcn_permlane32_swap(__float_as_uint(v), __float_as_uint(v), false, false); return __uint_as_float(r[0]) + __uint_as_float(r[1]); }
DI int lane_id() { int l; asm volatile("v_mbcnt_lo_u32_b32 %0, -1, 0\n\tv_mbcnt_hi_u32_b32 %0, -1, %0" : "=v"(l)); return l; }
DI float max3(float a, float b, float c) { float r; asm volatile("v_max3_f32 %0, %1, %2, %3" : "=v"(r) : "v"(a), "v"(b), "v"(c)); return r; }
DI unsigned opaque(unsigned v) { asm volatile("" : "+v"(v)); return v; }
#define MFMA32(a, b, c) __builtin_amdgcn_mfma_f32_32x32x16_bf16((a), (b), (c), 0, 0, 0)
#define MFMA16(a, b, c) __builtin_amdgcn_mfma_f32_16x16x32_bf16((a), (b), (c), 0, 0, 0)
#define SGB(mask, n) __builtin_amdgcn_sched_group_barrier((mask), (n), 0)
template <int I, int N, class F> DI void static_for(F&& f) { if constexpr (I < N) { f(std::integral_constant<int, I>{}); static_for<I + 1, N>(f); } }
template <int I> using IC = std::integral_constant<int, I>;

template <bool NTL> DI void dma4(unsigned lds, const char* g, unsigned v0, unsigned v1, unsigned v2, unsigned v3) {
  unsigned keep;
  if constexpr (NTL)
    asm volatile("s_mov_b32 %0, m0\n\ts_mov_b32 m0, %1\n\ts_nop 3\n\tglobal_load_lds_dwordx4 %3, %2 nt\n\t"
                 "s_add_u32 m0, m0, 0x400\n\ts_nop 0\n\tglobal_load_lds_dwordx4 %4, %2 nt\n\t"
                 "s_add_u32 m0, m0, 0x400\n\ts_nop 0\n\tglobal_load_lds_dwordx4 %5, %2 nt\n\t"
                 "s_add_u32 m0, m0, 0x400\n\ts_nop 0\n\tglobal_load_lds_dwordx4 %6, %2 nt\n\ts_mov_b32 m0, %0"
                 : "=&s"(keep) : "s"(lds), "s"(g), "v"(v0), "v"(v1), "v"(v2), "v"(v3) : "memory", "scc");
  else
    asm volatile("s_mov_b32 %0, m0\n\ts_mov_b32 m0, %1\n\ts_nop 3\n\tglobal_load_lds_dwordx4 %3, %2\n\t"
                 "s_add_u32 m0, m0, 0x400\n\ts_nop 0\n\tglobal_load_lds_dwordx4 %4, %2\n\t"
                 "s_add_u32 m0, m0, 0x400\n\ts_nop 0\n\tglobal_load_lds_dwordx4 %5, %2\n\t"
                 "s_add_u32 m0, m0, 0x400\n\ts_nop 0\n\tglobal_load_lds_dwordx4 %6, %2\n\ts_mov_b32 m0, %0"
                 : "=&s"(keep) : "s"(lds), "s"(g), "v"(v0), "v"(v1), "v"(v2), "v"(v3) : "memory", "scc");
}

DI void dma3(unsigned lds, const char* g, unsigned v0, unsigned v1, unsigned v2) {
  unsigned keep;
  asm volatile("s_mov_b32 %0, m0\n\ts_mov_b32 m0, %1\n\ts_nop 3\n\tglobal_load_lds_dwordx4 %3, %2\n\t"
               "s_add_u32 m0, m0, 0x400\n\ts_nop 0\n\tglobal_load_lds_dwordx4 %4, %2\n\t"
               "s_add_u32 m0, m0, 0x400\n\ts_nop 0\n\tglobal_load_lds_dwordx4 %5, %2\n\ts_mov_b32 m0, %0"
               : "=&s"(keep) : "s"(lds), "s"(g), "v"(v0), "v"(v1), "v"(v2) : "memory", "scc");
}

#define STAMP(i) do {} while (0)
#define STAMPL(i) do {} while (0)
#define CSTAMP(i) do {} while (0)

__global__ __launch_bounds__(512, 2) void fused_kernel(const u16* __restrict__ x, const u16* __restrict__ kc, const u16* __restrict__ vc,
                                                      const u16* __restrict__ wq, const u16* __restrict__ wo, u16* out, char* ws) {
  extern __shared__ __attribute__((aligned(16))) char smem[];
  const char* pc0;
  asm volatile("s_getpc_b64 %0" : "=s"(pc0));
  const int tid = threadIdx.x;
  const int wid = __builtin_amdgcn_readfirstlane(tid >> 6);
  const int bid = blockIdx.x, xcd = bid & 7, idx = bid >> 3;
  const int h = xcd * 4 + (idx & 3), b = idx >> 2;
  const int kh = xcd & 1, rb = 8 * (xcd >> 2) + (idx >> 2), cb = 4 * ((xcd >> 1) & 1) + (idx & 3);
  const int ks0 = kh * 64 + ((wid + (idx >> 2)) & 7) * 8;
  const int rot = (b + 2 * (idx & 3)) & 7;
  const unsigned sbase = (unsigned)(size_t)(LAS char*)smem;
  volatile LAS unsigned* ctl = (volatile LAS unsigned*)(size_t)(sbase + CTRL);
  u32x4 wf[32];
  u64 tag;
  const __amdgpu_buffer_rsrc_t ors = __builtin_amdgcn_make_buffer_rsrc(ws + WS_O, 0, (int)(O_BYTES + WT_BYTES), 0x00020000);
  const unsigned wt_off = (unsigned)O_BYTES + (unsigned)(cb * 128 + ks0) * 4096;
  unsigned done_seen = 0;
  auto wait_done = [&](unsigned need) AI {
    if (done_seen < need) {
#pragma unroll 1
      for (unsigned sp = 0; sp < SPIN_CAP; ++sp) {
        const u32x4 d = *(volatile LAS u32x4*)(size_t)(sbase + CTRL + 4 * W_DONE);
        done_seen = __builtin_amdgcn_readfirstlane(min(d.x, min(d.y, d.z)));
        if (done_seen >= need) break;
        __builtin_amdgcn_s_sleep(1);
      }
    }
    CFENCE();
  };
  auto wait_word = [&](int w) AI {
#pragma unroll 1
    for (unsigned sp = 0; sp < SPIN_CAP; ++sp) { if (__builtin_amdgcn_readfirstlane(ctl[w]) != 0u) break; __builtin_amdgcn_s_sleep(1); }
    CFENCE();
  };
  auto wait_landed = [&](unsigned n) AI {
#pragma unroll 1
    for (unsigned sp = 0; sp < SPIN_CAP; ++sp) { if ((__builtin_amdgcn_readfirstlane(ctl[W_FULL]) & 255u) >= n) break; __builtin_amdgcn_s_sleep(2); }
    CFENCE();
  };
  auto get_tag = [&]() AI { wait_word(W_TAG + 2); tag = (u64)ctl[W_TAG] | ((u64)ctl[W_TAG + 1] << 32); };

  if (__builtin_expect(wid >= 4, 1)) {
    const int lane = tid & 63, q = wid - 4;
    if (q == 3) {
      const unsigned co = min((unsigned)(idx * 8 + (lane & 7)) * 128u, (unsigned)CODE_BYTES);
      unsigned keep;
      asm volatile("s_mov_b32 %0, m0\n\ts_mov_b32 m0, %1\n\ts_nop 3\n\tglobal_load_lds_dword %3, %2\n\ts_mov_b32 m0, %0"
                   : "=&s"(keep) : "s"(sbase + SCR2 + 2048 + 512), "s"(pc0), "v"(co) : "memory");
    }
    const unsigned tstart = (unsigned)__builtin_amdgcn_s_memrealtime();
    const int r7 = lane >> 3, sl = lane & 7;
    const unsigned c0 = (sl ^ (((r7 & 2) << 1) | (r7 >> 2))) * 16, c1 = c0 ^ 32;
    const unsigned kv0 = r7 * 256 + c0, kv2 = (8 + r7) * 256 + c1, w0 = r7 * 8192 + c0, w2 = (8 + r7) * 8192 + c1;
    const char* kb = (const char*)kc + (size_t)(b * NH + h) * SK * DH * 2 + q * 4096;
    const char* vb = (const char*)vc + (size_t)(b * NH + h) * SK * DH * 2 + q * 4096;
    const char* wb = (const char*)wq + h * DH * 2 + q * 16 * 8192;
    const unsigned ldsq = sbase + q * 4096;
    volatile LAS unsigned char* fullb = (volatile LAS unsigned char*)(size_t)(sbase + CTRL + 4 * W_FULL);
    const char* xb = (const char*)x + (size_t)(b * SQ + q * 24) * (DM * 2);
    const unsigned xc = c0 ^ ((q & 1) * 32), x0 = r7 * 1024 + xc, x1 = (8 + r7) * 1024 + (xc ^ 32), x2 = x0 + 16 * 1024;
    auto ldx = [&](int j) AI { dma3(sbase + XREG + (j % 5) * XAREA + q * 3072, xb + ((j + rot) & 7) * 128, x0, x1, x2); };
    auto request = [&](auto N) AI {
      constexpr int n = N.value, c = tile_at(n);
      if constexpr (need_tab[c] > 0) wait_done(need_tab[c]);
      if constexpr (PACE_T > 0 && n >= 12) {
#pragma unroll 1
        while ((unsigned)__builtin_amdgcn_s_memrealtime() - tstart < (unsigned)(PACE_A + (n - 10) * PACE_T)) __builtin_amdgcn_s_sleep(2);
      }
      const unsigned lds = ldsq + slot_tab[c] * SLOTB;
      if constexpr (c < 8) {
        dma4<false>(lds, wb + (size_t)((c + rot) & 7) * 64 * 8192, w0, w0 + 128, w2, w2 + 128);
        if constexpr (c == 0) ldx(0);
        if constexpr (c < 7) ldx(c + 1);
        if constexpr (c == 0) bar();
      } else {
        constexpr bool isk = (c == 8) || ((c & 1) && c != 23);
        constexpr int ti = c == 8 ? 0 : (c == 23 ? 7 : ((c & 1) ? (c - 7) >> 1 : (c - 10) >> 1));
        constexpr bool ntl = isk ? K_NT != 0 : V_NT != 0;
        const char* g = (isk ? kb : vb) + ti * SLOTB;
        dma4<ntl>(lds, g, kv0, kv0 + 128, kv2, kv2 + 128);
        if constexpr (n >= 10) dma4<ntl>(lds + 8192, g + 8192, kv0, kv0 + 128, kv2, kv2 + 128);
      }
      if constexpr (n == 9) { wait_vm<inflight(9, n)>(); fullb[q] = 9; CFENCE(); STAMP(9); }
      if constexpr (pubto(n) > (n ? pubto(n - 1) : 0)) { wait_vm<inflight(pubto(n), n)>(); fullb[q] = (unsigned char)pubto(n); CFENCE(); STAMP(pubto(n)); }
    };
    static_for<0, 10>(request);
    if (q < 2) {
      static_for<10, NT>(request);
      static_for<0, WIN>([&](auto I) AI {
        constexpr int i = I.value;
        wait_vm<8 * (WIN - 1 - i)>(); fullb[q] = (unsigned char)(NT - WIN + i + 1); CFENCE(); STAMP(1 + NT - WIN + i);
      });
      wait_done(NT);
    } else {
      fullb[q] = 127;
      CFENCE();
      wait_landed(FREE_AT);
    }
    wait_word(W_WT);
#pragma unroll
    for (int e = 0; e < 32; ++e) wf[e] = __builtin_amdgcn_raw_buffer_load_b128(ors, wt_off + lane * 16, e * 1024, 16);
    wait_done(NT);
    get_tag();
    STAMP(33);
  } else if (wid == 3) {
    const int lane = tid & 63;
    const int ks = bid >> 1, half = bid & 1;
    {
      const char* src = (const char*)wo + (size_t)(ks * 32 + (lane >> 3)) * 1024 + half * 512 + (lane & 7) * 16;
      u32x4 raw[16];
      if (lane < 16) ctl[lane] = 0u;
      bar();
      if (AUX_LD > 0) wait_landed(AUX_LD);
      tag = __hip_atomic_load((gu64*)(ws + WS_EPOCH), RLX_AGENT);
#pragma unroll
      for (int j = 0; j < 4; ++j)
#pragma unroll
        for (int a = 0; a < 4; ++a) raw[4 * j + a] = __builtin_nontemporal_load((const u32x4*)(src + a * 8192 + j * 128));
      SB();
      const int g4 = lane >> 4, qq = (lane & 15) >> 2, p = lane & 3;
      const unsigned sc = sbase + SCR2, ta = sc + 128 * (4 * (g4 & 1) + qq) + 32 * (g4 >> 1) + 8 * p;
      const unsigned dst = (unsigned)O_BYTES + (unsigned)(half * 4 * 128 + ks) * 4096 + (lane >> 5) * 1024 + (lane & 31) * 16;
#pragma unroll
      for (int ps = 0; ps < 8; ++ps) {
        *(LAS u32x4*)(size_t)(sc + lane * 16) = raw[2 * ps];
        *(LAS u32x4*)(size_t)(sc + 1024 + lane * 16) = raw[2 * ps + 1];
        CFENCE();
#pragma unroll
        for (int m = 0; m < 2; ++m) raw[2 * ps + m] = __builtin_bit_cast(u32x4, tr2(ta + m * 64, ta + m * 64 + 1024));
        CFENCE();
      }
      if (AUX_ST > 0) wait_landed(AUX_ST);
#pragma unroll
      for (int e = 0; e < 16; ++e) __builtin_amdgcn_raw_buffer_store_b128(raw[e], ors, dst + ((e >> 1) & 1) * 512 + (e & 1) * 2048, (e >> 2) * 128 * 4096, 16);
      {
        const __amdgpu_buffer_rsrc_t urs = __builtin_amdgcn_make_buffer_rsrc(out, 0, NB * SQ * DM * 2, 0x00020000);
#pragma unroll
        for (int e = 0; e < 3; ++e) __builtin_amdgcn_raw_buffer_store_b128(u32x4{0u, 0u, 0u, 0u}, urs, bid * 3072 + lane * 16, e * 1024, 16);
      }
      ctl[W_TAG] = (unsigned)tag; ctl[W_TAG + 1] = (unsigned)(tag >> 32);
      CFENCE();
      ctl[W_TAG + 2] = 1u;
      wait_vm<0>();
      if (lane == 0) __hip_atomic_store((gu64*)(ws + WS_FLAG2) + half * 128 + ks, tag + TAG_STEP, RLX_AGENT);
    }
    STAMP(1);
    wait_done(AUX_AT);
    {
      const __amdgpu_buffer_rsrc_t frs = __builtin_amdgcn_make_buffer_rsrc(ws + WS_FLAG2, 0, 2048, 0x00020000);
      const unsigned tl = (unsigned)(tag + TAG_STEP), th = (unsigned)((tag + TAG_STEP) >> 32);
#pragma unroll 1
      for (unsigned sp = 0; sp < SPIN_CAP; ++sp) {
        const u32x4 v = __builtin_amdgcn_raw_buffer_load_b128(frs, lane * 16, 0, 16), w = __builtin_amdgcn_raw_buffer_load_b128(frs, lane * 16, 1024, 16);
        if (__all(v.x == tl && v.y == th && v.z == tl && v.w == th && w.x == tl && w.y == th && w.z == tl && w.w == th)) break;
        __builtin_amdgcn_s_sleep(32);
      }
      CFENCE();
      ctl[W_WT] = 1u;
      CFENCE();
    }
    STAMP(2);
    wait_landed(NT - WIN);
#pragma unroll
    for (int e = 0; e < 32; ++e) wf[e] = __builtin_amdgcn_raw_buffer_load_b128(ors, wt_off + lane * 16, e * 1024, 16);
    STAMP(33);
    wait_done(NT);
  } else {
    int lane = tid & 63;
    int r = lane & 31, hh = lane >> 5, blk = (lane >> 4) & 1, qq = (lane & 15) >> 2, p = lane & 3;
    const int ps = ((p & 1) << 1) | (p >> 1);
    bar();
    unsigned fl;
    const unsigned cta = opaque(sbase + CTRL), dna = opaque(sbase + CTRL + 4 * (W_DONE + wid));
    unsigned dnv = 0;
    auto lacking = [](unsigned v, int n) AI { return (((unsigned)__builtin_amdgcn_readfirstlane(v) - (unsigned)(n + 1) * 0x01010101u) & 0x80808080u) != 0u; };
    auto peek = [&]() AI { fl = *(volatile LAS unsigned*)(size_t)(cta + 4 * W_FULL); };
    auto wait_full = [&](int n) AI {
      if (__builtin_expect(lacking(fl, n), 0)) {
#pragma unroll 1
        for (unsigned sp = 0; sp < SPIN_CAP; ++sp) { if (!lacking(*(volatile LAS unsigned*)(size_t)(cta + 4 * W_FULL), n)) break; }
      }
      CFENCE();
    };
    auto release = [&]() AI { CFENCE(); dnv += 1; asm volatile("" : "+v"(dnv)); *(volatile LAS unsigned*)(size_t)dna = dnv; CFENCE(); };
    const unsigned wa = sbase + 2048 * hh + 128 * qq + 16 * ((2 * blk + (ps >> 1)) ^ (((qq & 2) << 1) | (2 * hh))) + 8 * (ps & 1);
    const unsigned va = sbase + 128 * (4 * hh + qq) + 16 * ((2 * blk + (p >> 1)) ^ (((qq & 2) << 1) | hh)) + 8 * (p & 1);
    const unsigned fr = 128 * (r & 7) + 16 * (hh ^ (((r & 2) << 1) | ((r >> 2) & 3)));
    const unsigned ka = sbase + 2048 * (r >> 3) + fr;
    const unsigned xa = sbase + XREG + 1024 * (4 * wid + (r >> 3)) + fr;
    f32x16 acc[4];
    bf16x8 FR[8];
    unsigned ab[4], kb4[4];
    auto setab = [&](unsigned (&d)[4], unsigned a, unsigned x1, unsigned x2) AI { d[0] = opaque(a); d[1] = opaque(a ^ x1); d[2] = opaque(a ^ x2); d[3] = opaque(a ^ x1 ^ x2); };
    auto seta = [&](unsigned (&d)[4], unsigned a, int j) AI { d[j] = opaque(j == 0 ? a : d[0] ^ (32 * j)); };
    auto rdw = [&](int i, int so) AI { const int ks = i >> 2, c = i & 3, o = so + 4096 * ks + 1024 * (c >> 1); FR[i & 7] = tr2(ab[2 * (c & 1)] + o, ab[2 * (c & 1) + 1] + o + 512); };
    auto rdv = [&](int i) AI { const int ks = i >> 2, c = i & 3, o = 4096 * ks + 1024 * (c >> 1); FR[i & 7] = tr2(ab[2 * (c & 1)] + o, ab[2 * (c & 1) + 1] + o + 2048); };
    auto rdk = [&](int i) AI { FR[i & 7] = *(LAS bf16x8*)(size_t)(kb4[i & 3] + 1024 * ((i >> 2) & 1) + 8192 * (i >> 3)); };
    auto ready = [&](int c) AI { wait_full(c); STAMPL(1 + c); SB(); };
    {
      bf16x8 XF[4];
      unsigned xb4[4] = {opaque(xa), opaque(xa ^ 32), opaque(xa ^ 64), opaque(xa ^ 96)};
      auto rdx = [&](int tile, int ks) AI { XF[ks] = *(LAS bf16x8*)(size_t)(xb4[ks] + (tile % 5) * XAREA); };
      setab(ab, wa, 16, 64);
      setab(kb4, ka + slot_tab[8] * SLOTB, 32, 64);
      peek(); wait_full(0); STAMP(1); CSTAMP(29);
      rdx(0, 0); rdx(0, 1);
#pragma unroll
      for (int i = 0; i < 6; ++i) { rdw(i ^ 1, 0); SB(); }
      SB();
      static_for<0, 8>([&](auto TL) AI {
        constexpr int tile = TL.value;
        static_for<0, 16>([&](auto I) AI {
          constexpr int i = I.value;
          if constexpr (i == 10) { wait_full(order_of(tile + 1)); STAMPL(2 + tile); SB(); }
          acc[i & 3] = MFMA32(FR[i & 7], XF[i >> 2], (tile == 0 && i < 4) ? f32x16{} : acc[i & 3]);
          SB();
          if constexpr ((i & 1) == 0) {
            if constexpr (i < 10) { rdw(i + 7, (slot_tab[tile] & 3) * SLOTB); SB(); rdw(i + 6, (slot_tab[tile] & 3) * SLOTB); }
            else if constexpr (tile < 7) { rdw(i - 9, (slot_tab[tile + 1] & 3) * SLOTB); SB(); rdw(i - 10, (slot_tab[tile + 1] & 3) * SLOTB); }
            else { rdk(i - 9); SB(); rdk(i - 10); }
          }
          if constexpr (i == 1) rdx(tile, 2);
          if constexpr (i == 5) rdx(tile, 3);
          if constexpr (i == 7) peek();
          if constexpr (i == 9) release();
          if constexpr (i == 11 && tile < 7) rdx(tile + 1, 0);
          if constexpr (i == 13 && tile < 7) rdx(tile + 1, 1);
          SB();
        });
      });
    }
    STAMP(9); CSTAMP(30);
    const float CS = 0.08838834764831845f * 1.4426950408889634f;
    bf16x8 qf[8];
#pragma unroll
    for (int c = 0; c < 4; ++c) { acc[c] *= CS; qf[2 * c] = pack8(acc[c], 0); qf[2 * c + 1] = pack8(acc[c], 1); }
#pragma unroll
    for (int c = 0; c < 4; ++c) acc[c] = f32x16{};
    float l = 0.f, alpha = 1.f, rs0 = 0.f, rs1 = 0.f, mxa, mxb;
    bool pend = false;
    f32x16 s0, s1, negm;
    float e0[16], e1[16];
    unsigned pw[16];
    auto pin_acc = [&]() AI { asm volatile("" : "+v"(acc[0]), "+v"(acc[1]), "+v"(acc[2]), "+v"(acc[3])); };
#define PIN(v) asm volatile("" : "+v"(v))
    auto ex0 = [&](int i) AI { e0[i] = __builtin_amdgcn_exp2f(s0[i]); PIN(e0[i]); };
    auto ex1 = [&](int i) AI { e1[i] = __builtin_amdgcn_exp2f(s1[i]); PIN(e1[i]); };
    auto ad0 = [&](int i) AI { rs0 += e0[i]; PIN(rs0); rs1 += e0[i + 1]; PIN(rs1); };
    auto ad1 = [&](int i) AI { rs0 += e1[i]; PIN(rs0); rs1 += e1[i + 1]; PIN(rs1); };
    auto pk0 = [&](int w) AI { pw[w] = cvtpk(e0[2 * w], e0[2 * w + 1]); PIN(pw[w]); };
    auto pk1 = [&](int w) AI { pw[8 + w] = cvtpk(e1[2 * w], e1[2 * w + 1]); PIN(pw[8 + w]); };
    auto pfrag = [&](int k) AI { u32x4 v = {pw[4 * k], pw[4 * k + 1], pw[4 * k + 2], pw[4 * k + 3]}; return __builtin_bit_cast(bf16x8, v); };
    auto mx0 = [&](int i) AI { float& m = (i & 1) ? mxb : mxa; m = i < 2 ? fmaxf(s0[2 * i], s0[2 * i + 1]) : fmaxf(fmaxf(m, s0[2 * i]), s0[2 * i + 1]); PIN(m); };
    auto mx1 = [&](int i) AI { float& m = (i & 1) ? mxb : mxa; m = fmaxf(fmaxf(m, s1[2 * i]), s1[2 * i + 1]); PIN(m); };
    auto shift_ref = [&](float d) AI {
#pragma unroll
      for (int e = 0; e < 16; ++e) { s0[e] -= d; s1[e] -= d; negm[e] -= d; }
    };
    auto decide = [&]() AI {
      const float mx = fmaxf(mxa, mxb);
      if (__builtin_expect(__any(mx > 8.f), 0)) {
        const float d = fmaxf(swap32max(mx), 0.f);
        shift_ref(d);
        alpha = __builtin_amdgcn_exp2f(-d);
        l = (l + rs0 + rs1) * alpha; rs0 = 0.f; rs1 = 0.f;
        pend = true;
      }
      SB();
    };
    auto rescale = [&]() AI {
      if (__builtin_expect(pend, 0)) {
#pragma unroll
        for (int cc = 0; cc < 4; ++cc) acc[cc] *= alpha;
        pend = false;
      }
      SB();
    };
    negm = f32x16{};
    static_for<0, 16>([&](auto I) AI {
      constexpr int i = I.value;
      if constexpr (i == 10) { ready(9); setab(kb4, ka + slot_tab[9] * SLOTB, 32, 64); SB(); }
      if constexpr (i < 8) s0 = MFMA32(FR[i & 7], qf[i & 7], (i == 0 ? negm : s0)); else s1 = MFMA32(FR[i & 7], qf[i & 7], (i == 8 ? negm : s1));
        if constexpr (i < 8) PIN(s0); else PIN(s1);
      SB();
      if constexpr ((i & 1) == 0) { rdk((i + 7) & 15); SB(); rdk((i + 6) & 15); }
      if constexpr (i == 7) peek();
      if constexpr (i == 9) release();
      if constexpr (i >= 11 && i < 15) { mx0(2 * (i - 11)); mx0(2 * (i - 11) + 1); }
      SB();
    });
#pragma unroll
    for (int i = 0; i < 8; ++i) mx1(i);
    shift_ref(swap32max(fmaxf(mxa, mxb)));
#pragma unroll
    for (int i = 0; i < 16; ++i) ex0(i);
#pragma unroll
    for (int i = 0; i < 8; ++i) { ad0(2 * i); pk0(i); }
#pragma unroll
    for (int i = 0; i < 4; ++i) ex1(i);
    SB();
    u64 seq = pack_slots(10);
#pragma unroll 1
    for (int c = 9; c < 23; c += 2) {
      rescale();
      static_for<0, 16>([&](auto I) AI {
        constexpr int i = I.value;
        if constexpr (i == 10) ready(c + 1);
        if constexpr (i < 8) s0 = MFMA32(FR[i & 7], qf[i & 7], (i == 0 ? negm : s0)); else s1 = MFMA32(FR[i & 7], qf[i & 7], (i == 8 ? negm : s1));
        if constexpr (i < 8) PIN(s0); else PIN(s1);
        SB();
        if constexpr ((i & 1) == 0) { if constexpr (i < 10) { rdk(i + 7); SB(); rdk(i + 6); } else { rdv(i - 9); SB(); rdv(i - 10); } }
        if constexpr (i == 0) { ex1(4); pk1(0); seta(ab, va + ((unsigned)seq & 15u) * SLOTB, 0); }
        if constexpr (i == 1) { ex1(5); ex1(6); ad1(0); }
        if constexpr (i == 2) { ex1(7); pk1(1); seta(ab, 0, 1); }
        if constexpr (i == 3) { ex1(8); ex1(9); ad1(2); }
        if constexpr (i == 4) { ex1(10); pk1(2); seta(ab, 0, 2); }
        if constexpr (i == 5) { ex1(11); ex1(12); ad1(4); }
        if constexpr (i == 6) { ex1(13); pk1(3); seta(ab, 0, 3); }
        if constexpr (i == 7) { ex1(14); ex1(15); peek(); }
        if constexpr (i == 8) { ad1(6); pk1(4); }
        if constexpr (i == 9) { release(); }
        if constexpr (i == 10) { pk1(5); }
        if constexpr (i == 11) { mx0(0); mx0(1); ad1(8); }
        if constexpr (i == 12) { pk1(6); }
        if constexpr (i == 13) { mx0(2); mx0(3); ad1(10); mx0(4); mx0(5); }
        if constexpr (i == 14) { pk1(7); }
        if constexpr (i == 15) { mx0(6); mx0(7); ad1(12); ad1(14); }
        SB();
      });
      static_for<0, 16>([&](auto I) AI {
        constexpr int i = I.value;
        if constexpr (i == 10) ready(c == 21 ? 21 : c + 2);
        acc[i & 3] = MFMA32(FR[i & 7], pfrag(i >> 2), acc[i & 3]);
        PIN(acc[i & 3]);
        SB();
        if constexpr ((i & 1) == 0) { if constexpr (i < 10) { rdv(i + 7); SB(); rdv(i + 6); } else { rdk(i - 9); SB(); rdk(i - 10); } }
        if constexpr (i == 0) { seta(kb4, ka + ((unsigned)(seq >> 4) & 15u) * SLOTB, 0); seq >>= 8; }
        if constexpr (i == 1) { mx1(0); mx1(1); mx1(2); mx1(3); mx1(4); mx1(5); }
        if constexpr (i == 2) { mx1(6); mx1(7); }
        if constexpr (i == 3) { decide(); ex0(0); ex0(1); }
        if constexpr (i == 4) { seta(kb4, 0, 1); }
        if constexpr (i == 5) { ex0(2); ex0(3); ad0(0); }
        if constexpr (i == 6) { ex0(4); seta(kb4, 0, 2); }
        if constexpr (i == 7) { ex0(5); ex0(6); ad0(2); peek(); }
        if constexpr (i == 8) { ex0(7); seta(kb4, 0, 3); }
        if constexpr (i == 9) { release(); ex0(8); }
        if constexpr (i == 10) { ex0(9); ad0(4); pk0(0); }
        if constexpr (i == 11) { ex0(10); ex0(11); ad0(6); pk0(1); pk0(2); }
        if constexpr (i == 12) { ex0(12); ex0(13); pk0(3); pk0(4); }
        if constexpr (i == 13) { ex0(14); ex0(15); ad0(8); pk0(5); }
        if constexpr (i == 14) { ex1(0); ex1(1); ad0(10); pk0(6); }
        if constexpr (i == 15) { ex1(2); ex1(3); ad0(12); ad0(14); pk0(7); }
        SB();
      });
    }
    rescale();
#pragma unroll
    for (int i = 4; i < 16; ++i) ex1(i);
#pragma unroll
    for (int i = 0; i < 8; ++i) { ad1(2 * i); pk1(i); }
    const float linv = 1.f / swap32sum(l + rs0 + rs1);
    get_tag();
    peek();
    wait_full(23);
    STAMP(24); CSTAMP(31);
    setab(ab, va + slot_tab[23] * SLOTB, 32, 64);
#pragma unroll
    for (int i = 0; i < 6; ++i) rdv(i);
    SB();
    static_for<0, 16>([&](auto I) AI {
      constexpr int i = I.value;
      acc[i & 3] = MFMA32(FR[i & 7], pfrag(i >> 2), acc[i & 3]);
      SB();
      if constexpr ((i & 1) == 0 && i < 10) { rdv(i + 7); SB(); rdv(i + 6); }
      SB();
    });
    STAMP(25);
    lane = lane_id(); r = lane & 31; hh = lane >> 5;
    const int u = 2 * wid + (r >> 4);
    const unsigned ob = (((b * 6 + u) * 128 + h * 4) * 64 + hh * 16 + (r & 15)) * 16;
#pragma unroll
    for (int c = 0; c < 4; ++c) {
#pragma unroll
      for (int e = 0; e < 16; ++e) acc[c][e] *= linv;
      __builtin_amdgcn_raw_buffer_store_b128(__builtin_bit_cast(u32x4, pack8(acc[c], 0)), ors, ob + c * 1024, 0, 16);
      __builtin_amdgcn_raw_buffer_store_b128(__builtin_bit_cast(u32x4, pack8(acc[c], 1)), ors, ob + c * 1024 + 512, 0, 16);
    }
    SB();
    release();
    STAMP(26);
    wait_vm<0>();
    STAMP(27);
    {
      unsigned old = 0;
      if (lane == 0) old = __hip_atomic_fetch_add((LAS unsigned*)(size_t)(sbase + CTRL + 4 * W_OCNT), 1u, RLX_WG);
      old = __builtin_amdgcn_readfirstlane(old);
      if (old == 2u && lane == 0) __hip_atomic_store((gu64*)(ws + WS_FLAGS) + (size_t)(b * NH + h) * FLAG_STRIDE, tag + TAG_STEP, RLX_AGENT);
      SB();
    }
    wait_word(W_WT);
#pragma unroll
    for (int e = 0; e < 32; ++e) wf[e] = __builtin_amdgcn_raw_buffer_load_b128(ors, wt_off + lane * 16, e * 1024, 16);
    wait_done(NT);
    STAMP(28);
  }
  tag += TAG_STEP;
  const int lane = lane_id();
  {
    gu64* fp = (gu64*)(ws + WS_FLAGS) + (size_t)((rb >> 1) * NH + (ks0 >> 2) + (lane & 1)) * FLAG_STRIDE;
#pragma unroll 1
    for (unsigned sp = 0; sp < SPIN_CAP; ++sp) {
      const u64 f = __hip_atomic_load(fp, RLX_AGENT);
      if (__all(f == tag)) break;
      __builtin_amdgcn_s_sleep(1);
    }
    CFENCE();
  }
  STAMP(34);
  f32x4 oa[4][3];
  {
    const unsigned obase = ((rb * 3) * 128 + ks0) * 1024 + lane * 16;
    u32x4 of[8][3];
    auto ldo = [&](int ks) AI {
#pragma unroll
      for (int u = 0; u < 3; ++u) of[ks][u] = __builtin_amdgcn_raw_buffer_load_b128(ors, obase + ks * 1024, u * 131072, 16);
    };
#pragma unroll
    for (int ks = 0; ks < 5; ++ks) ldo(ks);
    SB();
#pragma unroll
    for (int ks = 0; ks < 8; ++ks) {
#pragma unroll
      for (int u = 0; u < 3; ++u)
#pragma unroll
        for (int nb = 0; nb < 4; ++nb) oa[nb][u] = MFMA16(__builtin_bit_cast(bf16x8, wf[ks * 4 + nb]), __builtin_bit_cast(bf16x8, of[ks][u]), ks == 0 ? f32x4{} : oa[nb][u]);
      SB();
      if (ks == 0) { ldo(5); ldo(6); }
      if (ks == 1) ldo(7);
      SB();
    }
  }
  STAMP(35);
  LAS f32x4* red = (LAS f32x4*)(size_t)sbase;
#pragma unroll
  for (int nb = 0; nb < 4; ++nb)
#pragma unroll
    for (int u = 0; u < 3; ++u) red[(wid * 12 + nb * 3 + u) * 64 + lane] = oa[nb][u];
  bar();
  if (wid < 6) {
    const int u = wid >> 1, r8 = lane & 7, g4 = (lane >> 3) & 3, nl = lane >> 5;
    const int ri = (nl * 3 + u) * 64 + g4 * 16 + (wid & 1) * 8 + r8;
    f32x4 s0 = red[ri], s1 = red[ri + 6 * 64];
#pragma unroll
    for (int w = 1; w < 8; ++w) { s0 += red[w * 12 * 64 + ri]; s1 += red[w * 12 * 64 + ri + 6 * 64]; }
    const unsigned stg = sbase + 98304 + wid * 1024;
    *(LAS u32x2*)(size_t)(stg + r8 * 128 + nl * 32 + g4 * 8) = u32x2{cvtpk(s0[0], s0[1]), cvtpk(s0[2], s0[3])};
    *(LAS u32x2*)(size_t)(stg + r8 * 128 + 64 + nl * 32 + g4 * 8) = u32x2{cvtpk(s1[0], s1[1]), cvtpk(s1[2], s1[3])};
    CFENCE();
    GAS s16x2* op = (GAS s16x2*)(out + (size_t)(rb * 48 + wid * 8 + nl) * DM + cb * 64) + (lane & 31);
#pragma unroll
    for (int i = 0; i < 4; ++i) __builtin_amdgcn_global_atomic_fadd_v2bf16(op + i * DM, __builtin_bit_cast(s16x2, *(LAS unsigned*)(size_t)(stg + i * 256 + lane * 4)));
  } else if (wid == 7 && bid == 0) {
    bool ok = false;
#pragma unroll 1
    for (unsigned sp = 0; sp < SPIN_CAP && !ok; ++sp) {
      bool all = true;
#pragma unroll
      for (int i = 0; i < 4; ++i) all &= __hip_atomic_load((gu64*)(ws + WS_FLAGS) + (size_t)(i * 64 + lane) * FLAG_STRIDE, RLX_AGENT) == tag;
      ok = __all(all);
    }
    if (lane == 0) __hip_atomic_store((gu64*)(ws + WS_EPOCH), tag, RLX_AGENT);
  }
}

extern "C" void kernel_launch(void* const* d_in, const int* in_sizes, int n_in, void* d_out, int out_size, void* d_ws, size_t ws_size,
                              hipStream_t stream) {
  (void)hipFuncSetAttribute((const void*)fused_kernel, hipFuncAttributeMaxDynamicSharedMemorySize, LDS_BYTES);
  fused_kernel<<<256, 512, LDS_BYTES, stream>>>((const u16*)d_in[0], (const u16*)d_in[1], (const u16*)d_in[2], (const u16*)d_in[3],
                                                (const u16*)d_in[4], (u16*)d_out, (char*)d_ws);
}
```

```cpp
#include <hip/hip_runtime.h>
#include <hip/hip_bf16.h>
#include <utility>

#define LAS __attribute__((address_space(3)))
#define GAS __attribute__((address_space(1)))
#define DI __device__ __forceinline__
#define AI __attribute__((always_inline))
typedef __bf16 bf16x8 __attribute__((ext_vector_type(8)));
typedef __bf16 bf16x4 __attribute__((ext_vector_type(4)));
typedef __bf16 bf16x2 __attribute__((ext_vector_type(2)));
typedef float f32x16 __attribute__((ext_vector_type(16)));
typedef float f32x4 __attribute__((ext_vector_type(4)));
typedef float f32x2 __attribute__((ext_vector_type(2)));
typedef unsigned u32x4 __attribute__((ext_vector_type(4)));
typedef unsigned u32x2 __attribute__((ext_vector_type(2)));
typedef short s16x2 __attribute__((ext_vector_type(2)));
typedef unsigned short u16;
typedef unsigned long long u64;
typedef GAS u64 gu64;
#define RLX_AGENT __ATOMIC_RELAXED, __HIP_MEMORY_SCOPE_AGENT
#define RLX_WG __ATOMIC_RELAXED, __HIP_MEMORY_SCOPE_WORKGROUP

#define K_NT 1
#define V_NT 1
#define WIN 2
#define WINQ 1
#define AUX_LD 3
#define PACE_A 560
#define PACE_T 54
#define FREE_AT (NT - WIN)
#define CODE_BYTES 26400
#define RETOUCH 16
#define F0 (NT - WIN)
#define LWAIT 1
#define MAP48 0
#define POLL_SLEEP 1
#define CPRIO 0
#define KVROT 1
#define JITTER 54
#define JX 0
#define JQ 0
#define AUX_ST 0
#define WF_EARLY 0
#define AUX_AT 12

constexpr int NB = 8, SQ = 96, DM = 512, NH = 32, SK = 512, DH = 128;
constexpr int NSLOT = 9, SLOTB = 16384, NT = 24;
constexpr int XREG = 6 * SLOTB, XAREA = 12288;
constexpr int CTRL = XREG + 5 * XAREA;
constexpr int SCR2 = CTRL + 1024;
constexpr int LDS_BYTES = 163840;
constexpr int W_FULL = 0, W_DONE = 4, W_OCNT = 8, W_TAG = 12, W_WT = 15;
constexpr size_t WS_EPOCH = 0, WS_FLAGS = 4096, WS_FLAG2 = 40960, WS_O = 65536, O_BYTES = (size_t)NB * 6 * 128 * 1024, WS_WT = WS_O + O_BYTES, WT_BYTES = (size_t)NH * DH * DM * 2;
constexpr int FLAG_STRIDE = 16;
constexpr unsigned SPIN_CAP = 1u << 20;
constexpr u64 TAG_STEP = 0x9E3779B97F4A7C15ull;

constexpr int slot_tab[NT] = {0, 1, 2, 3, 0, 1, 2, 3, 4, 5, 0, 1, 2, 6, 3, 7, 8, 4, 5, 0, 1, 2, 6, 3};
constexpr int need_tab[NT] = {0, 0, 0, 0, 1, 2, 3, 4, 0, 0, 5, 6, 7, 0, 8, 0, 0, 9, 10, 11, 12, 13, 14, 15};
constexpr int tile_at(int n) { return n < 4 ? n : (n < 6 ? n + 4 : (n < 10 ? n - 2 : n)); }
constexpr int order_of(int c) { return c < 4 ? c : (c < 8 ? c + 2 : (c < 10 ? c - 4 : c)); }
constexpr int nops(int n) { return n == 0 ? 10 : (tile_at(n) < 7 ? 7 : (n < 10 ? 4 : 8)); }
constexpr int pub_tab[13] = {1, 1, 2, 3, 3, 4, 4, 7, 8, 9, 10, 10, 10};
constexpr int pubto(int n) { return n < 10 ? pub_tab[n] : (n + 1 - WIN > 10 ? n + 1 - WIN : 10); }
constexpr int inflight(int p, int n) { int s = (p == 1 ? 3 : 0); for (int i = p; i <= n; ++i) s += nops(i); return s; }
constexpr u64 pack_slots(int lo) { u64 v = 0; for (int i = 0; i < 16 && lo + i < NT; ++i) v |= (u64)slot_tab[lo + i] << (4 * i); return v; }
constexpr u64 SLOT_LO = pack_slots(0), SLOT_HI = pack_slots(16);
DI int slot_of(int c) { return (int)(((c < 16 ? SLOT_LO : SLOT_HI) >> (4 * (c & 15))) & 15); }

#define SB() __builtin_amdgcn_sched_barrier(0)
#define CFENCE() asm volatile("" ::: "memory")
template <int N> DI void wait_vm() { static_assert(N >= 0 && N < 64, "vmcnt"); asm volatile("s_waitcnt vmcnt(%0)" ::"n"(N) : "memory"); }
DI void bar() { asm volatile("s_waitcnt lgkmcnt(0)\n\ts_barrier" ::: "memory"); }
DI unsigned cvtpk(float lo, float hi) { f32x2 v = {lo, hi}; bf16x2 b = __builtin_convertvector(v, bf16x2); return __builtin_bit_cast(unsigned, b); }
DI bf16x8 pack8(const f32x16& x, int s) {
  u32x4 w = {cvtpk(x[8 * s], x[8 * s + 1]), cvtpk(x[8 * s + 2], x[8 * s + 3]), cvtpk(x[8 * s + 4], x[8 * s + 5]), cvtpk(x[8 * s + 6], x[8 * s + 7])};
  return __builtin_bit_cast(bf16x8, w);
}
DI bf16x4 trread(unsigned a) { return __builtin_amdgcn_ds_read_tr16_b64_v4bf16((LAS bf16x4*)(size_t)a); }
DI bf16x8 tr2(unsigned a0, unsigned a1) { bf16x4 lo = trread(a0), hi = trread(a1); return __builtin_shufflevector(lo, hi, 0, 1, 2, 3, 4, 5, 6, 7); }
DI float swap32max(float v) { auto r = __builtin_amdgcn_permlane32_swap(__float_as_uint(v), __float_as_uint(v), false, false); return fmaxf(__uint_as_float(r[0]), __uint_as_float(r[1])); }
DI float swap32sum(float v) { auto r = __builtin_amdgcn_permlane32_swap(__float_as_uint(v), __float_as_uint(v), false, false); return __uint_as_float(r[0]) + __uint_as_float(r[1]); }
DI int lane_id() { int l; asm volatile("v_mbcnt_lo_u32_b32 %0, -1, 0\n\tv_mbcnt_hi_u32_b32 %0, -1, %0" : "=v"(l)); return l; }
DI float max3(float a, float b, float c) { float r; asm volatile("v_max3_f32 %0, %1, %2, %3" : "=v"(r) : "v"(a), "v"(b), "v"(c)); return r; }
DI unsigned opaque(unsigned v) { asm volatile("" : "+v"(v)); return v; }
#define MFMA32(a, b, c) __builtin_amdgcn_mfma_f32_32x32x16_bf16((a), (b), (c), 0, 0, 0)
#define MFMA16(a, b, c) __builtin_amdgcn_mfma_f32_16x16x32_bf16((a), (b), (c), 0, 0, 0)
#define SGB(mask, n) __builtin_amdgcn_sched_group_barrier((mask), (n), 0)
template <int I, int N, class F> DI void static_for(F&& f) { if constexpr (I < N) { f(std::integral_constant<int, I>{}); static_for<I + 1, N>(f); } }
template <int I> using IC = std::integral_constant<int, I>;

template <bool NTL> DI void dma4(unsigned lds, const char* g, unsigned v0, unsigned v1, unsigned v2, unsigned v3) {
  unsigned keep;
  if constexpr (NTL)
    asm volatile("s_mov_b32 %0, m0\n\ts_mov_b32 m0, %1\n\ts_nop 3\n\tglobal_load_lds_dwordx4 %3, %2 nt\n\t"
                 "s_add_u32 m0, m0, 0x400\n\ts_nop 0\n\tglobal_load_lds_dwordx4 %4, %2 nt\n\t"
                 "s_add_u32 m0, m0, 0x400\n\ts_nop 0\n\tglobal_load_lds_dwordx4 %5, %2 nt\n\t"
                 "s_add_u32 m0, m0, 0x400\n\ts_nop 0\n\tglobal_load_lds_dwordx4 %6, %2 nt\n\ts_mov_b32 m0, %0"
                 : "=&s"(keep) : "s"(lds), "s"(g), "v"(v0), "v"(v1), "v"(v2), "v"(v3) : "memory", "scc");
  else
    asm volatile("s_mov_b32 %0, m0\n\ts_mov_b32 m0, %1\n\ts_nop 3\n\tglobal_load_lds_dwordx4 %3, %2\n\t"
                 "s_add_u32 m0, m0, 0x400\n\ts_nop 0\n\tglobal_load_lds_dwordx4 %4, %2\n\t"
                 "s_add_u32 m0, m0, 0x400\n\ts_nop 0\n\tglobal_load_lds_dwordx4 %5, %2\n\t"
                 "s_add_u32 m0, m0, 0x400\n\ts_nop 0\n\tglobal_load_lds_dwordx4 %6, %2\n\ts_mov_b32 m0, %0"
                 : "=&s"(keep) : "s"(lds), "s"(g), "v"(v0), "v"(v1), "v"(v2), "v"(v3) : "memory", "scc");
}

DI void dma3(unsigned lds, const char* g, unsigned v0, unsigned v1, unsigned v2) {
  unsigned keep;
  asm volatile("s_mov_b32 %0, m0\n\ts_mov_b32 m0, %1\n\ts_nop 3\n\tglobal_load_lds_dwordx4 %3, %2\n\t"
               "s_add_u32 m0, m0, 0x400\n\ts_nop 0\n\tglobal_load_lds_dwordx4 %4, %2\n\t"
               "s_add_u32 m0, m0, 0x400\n\ts_nop 0\n\tglobal_load_lds_dwordx4 %5, %2\n\ts_mov_b32 m0, %0"
               : "=&s"(keep) : "s"(lds), "s"(g), "v"(v0), "v"(v1), "v"(v2) : "memory", "scc");
}

#define STAMP(i) do {} while (0)
#define STAMPL(i) do {} while (0)
#define CSTAMP(i) do {} while (0)

__global__ __launch_bounds__(512, 2) void fused_kernel(const u16* __restrict__ x, const u16* __restrict__ kc, const u16* __restrict__ vc,
                                                      const u16* __restrict__ wq, const u16* __restrict__ wo, u16* out, char* ws) {
  extern __shared__ __attribute__((aligned(16))) char smem[];
  const char* pc0;
  asm volatile("s_getpc_b64 %0" : "=s"(pc0));
  const int tid = threadIdx.x;
  const int wid = __builtin_amdgcn_readfirstlane(tid >> 6);
  const int bid = blockIdx.x, xcd = bid & 7, idx = bid >> 3;
  const int h = xcd * 4 + (idx & 3), b = idx >> 2;
  const int kh = xcd & 1, rb = 8 * (xcd >> 2) + (idx >> 2), cb = 4 * ((xcd >> 1) & 1) + (idx & 3);
  const int ks0 = kh * 64 + ((wid + (idx >> 2)) & 7) * 8;
  const int rot = (b + 2 * (idx & 3)) & 7;
  const unsigned sbase = (unsigned)(size_t)(LAS char*)smem;
  volatile LAS unsigned* ctl = (volatile LAS unsigned*)(size_t)(sbase + CTRL);
  u32x4 wf[32];
  u64 tag;
  const __amdgpu_buffer_rsrc_t ors = __builtin_amdgcn_make_buffer_rsrc(ws + WS_O, 0, (int)(O_BYTES + WT_BYTES), 0x00020000);
  const unsigned wt_off = (unsigned)O_BYTES + (unsigned)(cb * 128 + ks0) * 4096;
  unsigned done_seen = 0;
  auto wait_done = [&](unsigned need) AI {
    if (done_seen < need) {
#pragma unroll 1
      for (unsigned sp = 0; sp < SPIN_CAP; ++sp) {
        const u32x4 d = *(volatile LAS u32x4*)(size_t)(sbase + CTRL + 4 * W_DONE);
        done_seen = __builtin_amdgcn_readfirstlane(min(d.x, min(d.y, d.z)));
        if (done_seen >= need) break;
        __builtin_amdgcn_s_sleep(1);
      }
    }
    CFENCE();
  };
  auto wait_word = [&](int w) AI {
#pragma unroll 1
    for (unsigned sp = 0; sp < SPIN_CAP; ++sp) { if (__builtin_amdgcn_readfirstlane(ctl[w]) != 0u) break; __builtin_amdgcn_s_sleep(1); }
    CFENCE();
  };
  auto wait_landed = [&](unsigned n) AI {
#pragma unroll 1
    for (unsigned sp = 0; sp < SPIN_CAP; ++sp) { if ((__builtin_amdgcn_readfirstlane(ctl[W_FULL]) & 255u) >= n) break; __builtin_amdgcn_s_sleep(2); }
    CFENCE();
  };
  auto get_tag = [&]() AI { wait_word(W_TAG + 2); tag = (u64)ctl[W_TAG] | ((u64)ctl[W_TAG + 1] << 32); };

  if (__builtin_expect(wid >= 4, 1)) {
    const int lane = tid & 63, q = wid - 4;
    if (q == 3) {
      const unsigned co = min((unsigned)(idx + 32 * (lane & 15)) * 64u, (unsigned)CODE_BYTES);
      unsigned keep;
      asm volatile("s_mov_b32 %0, m0\n\ts_mov_b32 m0, %1\n\ts_nop 3\n\tglobal_load_lds_dword %3, %2\n\ts_mov_b32 m0, %0"
                   : "=&s"(keep) : "s"(sbase + SCR2 + 2048 + 512), "s"(pc0), "v"(co) : "memory");
    }
    const unsigned tstart = (unsigned)__builtin_amdgcn_s_memrealtime() + (unsigned)(((idx * 11 + xcd * JX) & 31) * JITTER) / 32u - JITTER / 2 + (q & 1) * JQ;
    const int kvrot = KVROT == 1 ? (idx + xcd) & 7 : (KVROT == 2 ? (idx >> 2) ^ xcd : (KVROT == 3 ? ((idx >> 2) + 2 * (idx & 3) + xcd) & 7 : 0));
    const int r7 = lane >> 3, sl = lane & 7;
    const unsigned c0 = (sl ^ (((r7 & 2) << 1) | (r7 >> 2))) * 16, c1 = c0 ^ 32;
    const unsigned kv0 = r7 * 256 + c0, kv2 = (8 + r7) * 256 + c1, w0 = r7 * 8192 + c0, w2 = (8 + r7) * 8192 + c1;
    const char* kb = (const char*)kc + (size_t)(b * NH + h) * SK * DH * 2 + q * 4096;
    const char* vb = (const char*)vc + (size_t)(b * NH + h) * SK * DH * 2 + q * 4096;
    const char* wb = (const char*)wq + h * DH * 2 + q * 16 * 8192;
    const unsigned ldsq = sbase + q * 4096;
    volatile LAS unsigned char* fullb = (volatile LAS unsigned char*)(size_t)(sbase + CTRL + 4 * W_FULL);
    const char* xb = (const char*)x + (size_t)(b * SQ + q * 24) * (DM * 2);
    const unsigned xc = c0 ^ ((q & 1) * 32), x0 = r7 * 1024 + xc, x1 = (8 + r7) * 1024 + (xc ^ 32), x2 = x0 + 16 * 1024;
    auto ldx = [&](int j) AI { dma3(sbase + XREG + (j % 5) * XAREA + q * 3072, xb + ((j + rot) & 7) * 128, x0, x1, x2); };
    auto request = [&](auto N) AI {
      constexpr int n = N.value, c = tile_at(n);
      if constexpr (need_tab[c] > 0) wait_done(need_tab[c]);
      if constexpr (PACE_T > 0 && n >= 12) {
#pragma unroll 1
        while ((unsigned)__builtin_amdgcn_s_memrealtime() - tstart < (unsigned)(PACE_A + (n - 10) * PACE_T)) __builtin_amdgcn_s_sleep(2);
      }
      const unsigned lds = ldsq + slot_tab[c] * SLOTB;
      if constexpr (c < 8) {
        dma4<false>(lds, wb + (size_t)((c + rot) & 7) * 64 * 8192, w0, w0 + 128, w2, w2 + 128);
        if constexpr (c == 0) ldx(0);
        if constexpr (c < 7) ldx(c + 1);
        if constexpr (c == 0) bar();
      } else {
        constexpr bool isk = (c == 8) || ((c & 1) && c != 23);
        constexpr int ti = c == 8 ? 0 : (c == 23 ? 7 : ((c & 1) ? (c - 7) >> 1 : (c - 10) >> 1));
        constexpr bool ntl = isk ? K_NT != 0 : V_NT != 0;
        const char* g = (isk ? kb : vb) + ((ti + kvrot) & 7) * SLOTB;
        dma4<ntl>(lds, g, kv0, kv0 + 128, kv2, kv2 + 128);
        if constexpr (n >= 10) dma4<ntl>(lds + 8192, g + 8192, kv0, kv0 + 128, kv2, kv2 + 128);
      }
      if constexpr (pubto(n) > (n ? pubto(n - 1) : 0)) { wait_vm<inflight(pubto(n), n)>(); fullb[q] = (unsigned char)pubto(n); CFENCE(); STAMP(pubto(n)); }
    };
    static_for<0, 10>(request);
    if (q < 2) {
      static_for<10, NT>(request);
      static_for<0, WIN>([&](auto I) AI {
        constexpr int i = I.value;
        wait_vm<8 * (WIN - 1 - i)>(); fullb[q] = (unsigned char)(NT - WIN + i + 1); CFENCE(); STAMP(1 + NT - WIN + i);
      });
      if (LWAIT) wait_done(NT);
    } else {
      wait_vm<0>();
      fullb[q] = 127;
      CFENCE();
      wait_landed(FREE_AT);
    }
    wait_word(W_WT);
#pragma unroll
    for (int e = 0; e < 32; ++e) wf[e] = __builtin_amdgcn_raw_buffer_load_b128(ors, wt_off + lane * 16, e * 1024, 16);
    wait_done(NT);
    get_tag();
    STAMP(33);
  } else if (wid == 3) {
    const int lane = tid & 63;
    const int ks = bid >> 1, half = bid & 1;
    {
      const char* src = (const char*)wo + (size_t)(ks * 32 + (lane >> 3)) * 1024 + half * 512 + (lane & 7) * 16;
      u32x4 raw[16];
      if (lane < 16) ctl[lane] = 0u;
      bar();
      if (AUX_LD > 0) wait_landed(AUX_LD);
      tag = __hip_atomic_load((gu64*)(ws + WS_EPOCH), RLX_AGENT);
#pragma unroll
      for (int j = 0; j < 4; ++j)
#pragma unroll
        for (int a = 0; a < 4; ++a) raw[4 * j + a] = __builtin_nontemporal_load((const u32x4*)(src + a * 8192 + j * 128));
      SB();
      const int g4 = lane >> 4, qq = (lane & 15) >> 2, p = lane & 3;
      const unsigned sc = sbase + SCR2, ta = sc + 128 * (4 * (g4 & 1) + qq) + 32 * (g4 >> 1) + 8 * p;
      const unsigned dst = (unsigned)O_BYTES + (unsigned)(half * 4 * 128 + ks) * 4096 + (lane >> 5) * 1024 + (lane & 31) * 16;
#pragma unroll
      for (int ps = 0; ps < 8; ++ps) {
        *(LAS u32x4*)(size_t)(sc + lane * 16) = raw[2 * ps];
        *(LAS u32x4*)(size_t)(sc + 1024 + lane * 16) = raw[2 * ps + 1];
        CFENCE();
#pragma unroll
        for (int m = 0; m < 2; ++m) raw[2 * ps + m] = __builtin_bit_cast(u32x4, tr2(ta + m * 64, ta + m * 64 + 1024));
        CFENCE();
      }
      if (AUX_ST > 0) wait_landed(AUX_ST);
#pragma unroll
      for (int e = 0; e < 16; ++e) __builtin_amdgcn_raw_buffer_store_b128(raw[e], ors, dst + ((e >> 1) & 1) * 512 + (e & 1) * 2048, (e >> 2) * 128 * 4096, 16);
      {
        const __amdgpu_buffer_rsrc_t urs = __builtin_amdgcn_make_buffer_rsrc(out, 0, NB * SQ * DM * 2, 0x00020000);
#pragma unroll
        for (int e = 0; e < 3; ++e) __builtin_amdgcn_raw_buffer_store_b128(u32x4{0u, 0u, 0u, 0u}, urs, bid * 3072 + lane * 16, e * 1024, 16);
      }
      ctl[W_TAG] = (unsigned)tag; ctl[W_TAG + 1] = (unsigned)(tag >> 32);
      CFENCE();
      ctl[W_TAG + 2] = 1u;
      wait_vm<0>();
      if (lane == 0) __hip_atomic_store((gu64*)(ws + WS_FLAG2) + half * 128 + ks, tag + TAG_STEP, RLX_AGENT);
    }
    STAMP(1);
    wait_done(AUX_AT);
    {
      const __amdgpu_buffer_rsrc_t frs = __builtin_amdgcn_make_buffer_rsrc(ws + WS_FLAG2, 0, 2048, 0x00020000);
      const unsigned tl = (unsigned)(tag + TAG_STEP), th = (unsigned)((tag + TAG_STEP) >> 32);
#pragma unroll 1
      for (unsigned sp = 0; sp < SPIN_CAP; ++sp) {
        const u32x4 v = __builtin_amdgcn_raw_buffer_load_b128(frs, lane * 16, 0, 16), w = __builtin_amdgcn_raw_buffer_load_b128(frs, lane * 16, 1024, 16);
        if (__all(v.x == tl && v.y == th && v.z == tl && v.w == th && w.x == tl && w.y == th && w.z == tl && w.w == th)) break;
        __builtin_amdgcn_s_sleep(32);
      }
      CFENCE();
      ctl[W_WT] = 1u;
      CFENCE();
    }
    STAMP(2);
    if (RETOUCH > 0) {
      wait_landed(RETOUCH);
      const unsigned co = min((unsigned)(idx + 32 * (lane & 15)) * 64u, (unsigned)CODE_BYTES);
      unsigned keep;
      asm volatile("s_mov_b32 %0, m0\n\ts_mov_b32 m0, %1\n\ts_nop 3\n\tglobal_load_lds_dword %3, %2\n\ts_mov_b32 m0, %0"
                   : "=&s"(keep) : "s"(sbase + SCR2 + 2048 + 512), "s"(pc0), "v"(co) : "memory");
    }
    wait_landed(F0);
#pragma unroll
    for (int e = 0; e < 32; ++e) wf[e] = __builtin_amdgcn_raw_buffer_load_b128(ors, wt_off + lane * 16, e * 1024, 16);
    STAMP(33);
    wait_done(NT);
  } else {
    if (CPRIO) __builtin_amdgcn_s_setprio(CPRIO);
    int lane = tid & 63;
    int r = lane & 31, hh = lane >> 5, blk = (lane >> 4) & 1, qq = (lane & 15) >> 2, p = lane & 3;
    const int ps = ((p & 1) << 1) | (p >> 1);
    bar();
    unsigned fl;
    const unsigned cta = opaque(sbase + CTRL), dna = opaque(sbase + CTRL + 4 * (W_DONE + wid));
    unsigned dnv = 0;
    auto lacking = [](unsigned v, int n) AI { return (((unsigned)__builtin_amdgcn_readfirstlane(v) - (unsigned)(n + 1) * 0x01010101u) & 0x80808080u) != 0u; };
    auto peek = [&]() AI { fl = *(volatile LAS unsigned*)(size_t)(cta + 4 * W_FULL); };
    auto wait_full = [&](int n) AI {
      if (__builtin_expect(lacking(fl, n), 0)) {
#pragma unroll 1
        for (unsigned sp = 0; sp < SPIN_CAP; ++sp) { if (!lacking(*(volatile LAS unsigned*)(size_t)(cta + 4 * W_FULL), n)) break; }
      }
      CFENCE();
    };
    auto release = [&]() AI { CFENCE(); dnv += 1; asm volatile("" : "+v"(dnv)); *(volatile LAS unsigned*)(size_t)dna = dnv; CFENCE(); };
    const unsigned wa = sbase + 2048 * hh + 128 * qq + 16 * ((2 * blk + (ps >> 1)) ^ (((qq & 2) << 1) | (2 * hh))) + 8 * (ps & 1);
    const unsigned va = sbase + 128 * (4 * hh + qq) + 16 * ((2 * blk + (p >> 1)) ^ (((qq & 2) << 1) | hh)) + 8 * (p & 1);
    const unsigned fr = 128 * (r & 7) + 16 * (hh ^ (((r & 2) << 1) | ((r >> 2) & 3)));
    const unsigned ka = sbase + 2048 * (r >> 3) + fr;
    const unsigned xa = sbase + XREG + 1024 * (4 * wid + (r >> 3)) + fr;
    f32x16 acc[4];
    bf16x8 FR[8];
    unsigned ab[4], kb4[4];
    auto setab = [&](unsigned (&d)[4], unsigned a, unsigned x1, unsigned x2) AI { d[0] = opaque(a); d[1] = opaque(a ^ x1); d[2] = opaque(a ^ x2); d[3] = opaque(a ^ x1 ^ x2); };
    auto seta = [&](unsigned (&d)[4], unsigned a, int j) AI { d[j] = opaque(j == 0 ? a : d[0] ^ (32 * j)); };
    auto rdw = [&](int i, int so) AI { const int ks = i >> 2, c = i & 3, o = so + 4096 * ks + 1024 * (c >> 1); FR[i & 7] = tr2(ab[2 * (c & 1)] + o, ab[2 * (c & 1) + 1] + o + 512); };
    auto rdv = [&](int i) AI { const int ks = i >> 2, c = i & 3, o = 4096 * ks + 1024 * (c >> 1); FR[i & 7] = tr2(ab[2 * (c & 1)] + o, ab[2 * (c & 1) + 1] + o + 2048); };
    auto rdk = [&](int i) AI { FR[i & 7] = *(LAS bf16x8*)(size_t)(kb4[i & 3] + 1024 * ((i >> 2) & 1) + 8192 * (i >> 3)); };
    auto ready = [&](int c) AI { wait_full(c); STAMPL(1 + c); SB(); };
    {
      bf16x8 XF[4];
      unsigned xb4[4] = {opaque(xa), opaque(xa ^ 32), opaque(xa ^ 64), opaque(xa ^ 96)};
      auto rdx = [&](int tile, int ks) AI { XF[ks] = *(LAS bf16x8*)(size_t)(xb4[ks] + (tile % 5) * XAREA); };
      setab(ab, wa, 16, 64);
      setab(kb4, ka + slot_tab[8] * SLOTB, 32, 64);
      peek(); wait_full(0); STAMP(1);
      rdx(0, 0); rdx(0, 1);
#pragma unroll
      for (int i = 0; i < 6; ++i) { rdw(i ^ 1, 0); SB(); }
      SB();
      static_for<0, 8>([&](auto TL) AI {
        constexpr int tile = TL.value;
        static_for<0, 16>([&](auto I) AI {
          constexpr int i = I.value;
          if constexpr (i == 10) { wait_full(order_of(tile + 1)); STAMPL(2 + tile); SB(); }
          acc[i & 3] = MFMA32(FR[i & 7], XF[i >> 2], (tile == 0 && i < 4) ? f32x16{} : acc[i & 3]);
          SB();
          if constexpr ((i & 1) == 0) {
            if constexpr (i < 10) { rdw(i + 7, (slot_tab[tile] & 3) * SLOTB); SB(); rdw(i + 6, (slot_tab[tile] & 3) * SLOTB); }
            else if constexpr (tile < 7) { rdw(i - 9, (slot_tab[tile + 1] & 3) * SLOTB); SB(); rdw(i - 10, (slot_tab[tile + 1] & 3) * SLOTB); }
            else { rdk(i - 9); SB(); rdk(i - 10); }
          }
          if constexpr (i == 1) rdx(tile, 2);
          if constexpr (i == 5) rdx(tile, 3);
          if constexpr (i == 7) peek();
          if constexpr (i == 9) release();
          if constexpr (i == 11 && tile < 7) rdx(tile + 1, 0);
          if constexpr (i == 13 && tile < 7) rdx(tile + 1, 1);
          SB();
        });
      });
    }
    STAMP(9);
    const float CS = 0.08838834764831845f * 1.4426950408889634f;
    bf16x8 qf[8];
#pragma unroll
    for (int c = 0; c < 4; ++c) { acc[c] *= CS; qf[2 * c] = pack8(acc[c], 0); qf[2 * c + 1] = pack8(acc[c], 1); }
#pragma unroll
    for (int c = 0; c < 4; ++c) acc[c] = f32x16{};
    float l = 0.f, alpha = 1.f, rs0 = 0.f, rs1 = 0.f, mxa, mxb;
    bool pend = false;
    f32x16 s0, s1, negm;
    float e0[16], e1[16];
    unsigned pw[16];
    auto pin_acc = [&]() AI { asm volatile("" : "+v"(acc[0]), "+v"(acc[1]), "+v"(acc[2]), "+v"(acc[3])); };
#define PIN(v) asm volatile("" : "+v"(v))
    auto ex0 = [&](int i) AI { e0[i] = __builtin_amdgcn_exp2f(s0[i]); PIN(e0[i]); };
    auto ex1 = [&](int i) AI { e1[i] = __builtin_amdgcn_exp2f(s1[i]); PIN(e1[i]); };
    auto ad0 = [&](int i) AI { rs0 += e0[i]; PIN(rs0); rs1 += e0[i + 1]; PIN(rs1); };
    auto ad1 = [&](int i) AI { rs0 += e1[i]; PIN(rs0); rs1 += e1[i + 1]; PIN(rs1); };
    auto pk0 = [&](int w) AI { pw[w] = cvtpk(e0[2 * w], e0[2 * w + 1]); PIN(pw[w]); };
    auto pk1 = [&](int w) AI { pw[8 + w] = cvtpk(e1[2 * w], e1[2 * w + 1]); PIN(pw[8 + w]); };
    auto pfrag = [&](int k) AI { u32x4 v = {pw[4 * k], pw[4 * k + 1], pw[4 * k + 2], pw[4 * k + 3]}; return __builtin_bit_cast(bf16x8, v); };
    auto mx0 = [&](int i) AI { float& m = (i & 1) ? mxb : mxa; m = i < 2 ? fmaxf(s0[2 * i], s0[2 * i + 1]) : fmaxf(fmaxf(m, s0[2 * i]), s0[2 * i + 1]); PIN(m); };
    auto mx1 = [&](int i) AI { float& m = (i & 1) ? mxb : mxa; m = fmaxf(fmaxf(m, s1[2 * i]), s1[2 * i + 1]); PIN(m); };
    auto shift_ref = [&](float d) AI {
#pragma unroll
      for (int e = 0; e < 16; ++e) { s0[e] -= d; s1[e] -= d; negm[e] -= d; }
    };
    auto decide = [&]() AI {
      const float mx = fmaxf(mxa, mxb);
      if (__builtin_expect(__any(mx > 8.f), 0)) {
        const float d = fmaxf(swap32max(mx), 0.f);
        shift_ref(d);
        alpha = __builtin_amdgcn_exp2f(-d);
        l = (l + rs0 + rs1) * alpha; rs0 = 0.f; rs1 = 0.f;
        pend = true;
      }
      SB();
    };
    auto rescale = [&]() AI {
      if (__builtin_expect(pend, 0)) {
#pragma unroll
        for (int cc = 0; cc < 4; ++cc) acc[cc] *= alpha;
        pend = false;
      }
      SB();
    };
    negm = f32x16{};
    static_for<0, 16>([&](auto I) AI {
      constexpr int i = I.value;
      if constexpr (i == 10) { ready(9); setab(kb4, ka + slot_tab[9] * SLOTB, 32, 64); SB(); }
      if constexpr (i < 8) s0 = MFMA32(FR[i & 7], qf[i & 7], (i == 0 ? negm : s0)); else s1 = MFMA32(FR[i & 7], qf[i & 7], (i == 8 ? negm : s1));
        if constexpr (i < 8) PIN(s0); else PIN(s1);
      SB();
      if constexpr ((i & 1) == 0) { rdk((i + 7) & 15); SB(); rdk((i + 6) & 15); }
      if constexpr (i == 7) peek();
      if constexpr (i == 9) release();
      if constexpr (i >= 11 && i < 15) { mx0(2 * (i - 11)); mx0(2 * (i - 11) + 1); }
      SB();
    });
#pragma unroll
    for (int i = 0; i < 8; ++i) mx1(i);
    shift_ref(swap32max(fmaxf(mxa, mxb)));
#pragma unroll
    for (int i = 0; i < 16; ++i) ex0(i);
#pragma unroll
    for (int i = 0; i < 8; ++i) { ad0(2 * i); pk0(i); }
#pragma unroll
    for (int i = 0; i < 4; ++i) ex1(i);
    SB();
    u64 seq = pack_slots(10);
#pragma unroll 1
    for (int c = 9; c < 21; c += 2) {
      rescale();
      static_for<0, 16>([&](auto I) AI {
        constexpr int i = I.value;
        if constexpr (i == 10) ready(c + 1);
        if constexpr (i < 8) s0 = MFMA32(FR[i & 7], qf[i & 7], (i == 0 ? negm : s0)); else s1 = MFMA32(FR[i & 7], qf[i & 7], (i == 8 ? negm : s1));
        if constexpr (i < 8) PIN(s0); else PIN(s1);
        SB();
        if constexpr ((i & 1) == 0) { if constexpr (i < 10) { rdk(i + 7); SB(); rdk(i + 6); } else { rdv(i - 9); SB(); rdv(i - 10); } }
        if constexpr (i == 0) { ex1(4); pk1(0); seta(ab, va + ((unsigned)seq & 15u) * SLOTB, 0); }
        if constexpr (i == 1) { ex1(5); ex1(6); ad1(0); }
        if constexpr (i == 2) { ex1(7); pk1(1); seta(ab, 0, 1); }
        if constexpr (i == 3) { ex1(8); ex1(9); ad1(2); }
        if constexpr (i == 4) { ex1(10); pk1(2); seta(ab, 0, 2); }
        if constexpr (i == 5) { ex1(11); ex1(12); ad1(4); }
        if constexpr (i == 6) { ex1(13); pk1(3); seta(ab, 0, 3); }
        if constexpr (i == 7) { ex1(14); ex1(15); peek(); }
        if constexpr (i == 8) { ad1(6); pk1(4); }
        if constexpr (i == 9) { release(); }
        if constexpr (i == 10) { pk1(5); }
        if constexpr (i == 11) { mx0(0); mx0(1); ad1(8); }
        if constexpr (i == 12) { pk1(6); }
        if constexpr (i == 13) { mx0(2); mx0(3); ad1(10); mx0(4); mx0(5); }
        if constexpr (i == 14) { pk1(7); }
        if constexpr (i == 15) { mx0(6); mx0(7); ad1(12); ad1(14); }
        SB();
      });
      static_for<0, 16>([&](auto I) AI {
        constexpr int i = I.value;
        if constexpr (i == 10) ready(c + 2);
        acc[i & 3] = MFMA32(FR[i & 7], pfrag(i >> 2), acc[i & 3]);
        PIN(acc[i & 3]);
        SB();
        if constexpr ((i & 1) == 0) { if constexpr (i < 10) { rdv(i + 7); SB(); rdv(i + 6); } else { rdk(i - 9); SB(); rdk(i - 10); } }
        if constexpr (i == 0) { seta(kb4, ka + ((unsigned)(seq >> 4) & 15u) * SLOTB, 0); seq >>= 8; }
        if constexpr (i == 1) { mx1(0); mx1(1); mx1(2); mx1(3); mx1(4); mx1(5); }
        if constexpr (i == 2) { mx1(6); mx1(7); }
        if constexpr (i == 3) { decide(); ex0(0); ex0(1); }
        if constexpr (i == 4) { seta(kb4, 0, 1); }
        if constexpr (i == 5) { ex0(2); ex0(3); ad0(0); }
        if constexpr (i == 6) { ex0(4); seta(kb4, 0, 2); }
        if constexpr (i == 7) { ex0(5); ex0(6); ad0(2); peek(); }
        if constexpr (i == 8) { ex0(7); seta(kb4, 0, 3); }
        if constexpr (i == 9) { release(); ex0(8); }
        if constexpr (i == 10) { ex0(9); ad0(4); pk0(0); }
        if constexpr (i == 11) { ex0(10); ex0(11); ad0(6); pk0(1); pk0(2); }
        if constexpr (i == 12) { ex0(12); ex0(13); pk0(3); pk0(4); }
        if constexpr (i == 13) { ex0(14); ex0(15); ad0(8); pk0(5); }
        if constexpr (i == 14) { ex1(0); ex1(1); ad0(10); pk0(6); }
        if constexpr (i == 15) { ex1(2); ex1(3); ad0(12); ad0(14); pk0(7); }
        SB();
      });
    }
    rescale();
    static_for<0, 16>([&](auto I) AI {
      constexpr int i = I.value;
      if constexpr (i < 8) s0 = MFMA32(FR[i & 7], qf[i & 7], (i == 0 ? negm : s0)); else s1 = MFMA32(FR[i & 7], qf[i & 7], (i == 8 ? negm : s1));
      if constexpr (i < 8) PIN(s0); else PIN(s1);
      SB();
      if constexpr ((i & 1) == 0 && i < 10) { rdk(i + 7); SB(); rdk(i + 6); }
      if constexpr (i == 0) { ex1(4); pk1(0); seta(ab, va + slot_tab[22] * SLOTB, 0); }
      if constexpr (i == 1) { ex1(5); ex1(6); ad1(0); }
      if constexpr (i == 2) { ex1(7); pk1(1); seta(ab, 0, 1); }
      if constexpr (i == 3) { ex1(8); ex1(9); ad1(2); }
      if constexpr (i == 4) { ex1(10); pk1(2); seta(ab, 0, 2); }
      if constexpr (i == 5) { ex1(11); ex1(12); ad1(4); }
      if constexpr (i == 6) { ex1(13); pk1(3); seta(ab, 0, 3); }
      if constexpr (i == 7) { ex1(14); ex1(15); }
      if constexpr (i == 8) { ad1(6); pk1(4); }
      if constexpr (i == 9) { release(); ad1(8); pk1(5); }
      if constexpr (i == 10) { ad1(10); pk1(6); pk1(7); }
      if constexpr (i == 11) { mx0(0); mx0(1); ad1(12); }
      if constexpr (i == 12) { mx0(2); mx0(3); ad1(14); }
      if constexpr (i == 13) { mx0(4); mx0(5); mx0(6); mx0(7); }
      if constexpr (i == 14) peek();
      SB();
    });
    get_tag();
    unsigned p7[16];
    float oscale;
    auto softmax7 = [&]() AI {
#pragma unroll
      for (int i = 0; i < 8; ++i) mx1(i);
      decide();
#pragma unroll
      for (int i = 0; i < 16; ++i) { ex0(i); ex1(i); }
#pragma unroll
      for (int i = 0; i < 8; ++i) { ad0(2 * i); ad1(2 * i); }
      const float linv = __builtin_amdgcn_rcpf(swap32sum(l + rs0 + rs1));
#pragma unroll
      for (int i = 0; i < 8; ++i) { p7[i] = cvtpk(e0[2 * i] * linv, e0[2 * i + 1] * linv); PIN(p7[i]); p7[8 + i] = cvtpk(e1[2 * i] * linv, e1[2 * i + 1] * linv); PIN(p7[8 + i]); }
      oscale = pend ? alpha * linv : linv;
      PIN(oscale);
      SB();
    };
    softmax7();
    lane = lane_id(); r = lane & 31; hh = lane >> 5;
    const unsigned ob = opaque((((b * 6 + 2 * wid + (r >> 4)) * 128 + h * 4) * 64 + hh * 16 + (r & 15)) * 16);
    ready(22);
#pragma unroll
    for (int i = 0; i < 6; ++i) { rdv(i ^ 1); SB(); }
    static_for<0, 16>([&](auto I) AI {
      constexpr int i = I.value;
      acc[i & 3] = MFMA32(FR[i & 7], pfrag(i >> 2), acc[i & 3]);
      PIN(acc[i & 3]);
      SB();
      if constexpr ((i & 1) == 0 && i < 10) { rdv(i + 7); SB(); rdv(i + 6); }
      if constexpr (i == 9) release();
      if constexpr (i == 11) { setab(kb4, va + slot_tab[23] * SLOTB, 32, 64); peek(); }
      SB();
    });
#pragma unroll
    for (int cc = 0; cc < 4; ++cc) acc[cc] *= oscale;
    pin_acc();
    SB();
    wait_full(23);
    STAMP(24);
    auto rdv7 = [&](int i) AI { const int ks = i & 3, c = i >> 2, o = 4096 * ks + 1024 * (c >> 1); FR[i & 7] = tr2(kb4[2 * (c & 1)] + o, kb4[2 * (c & 1) + 1] + o + 2048); };
    auto sto = [&](int c, int hf) AI { __builtin_amdgcn_raw_buffer_store_b128(__builtin_bit_cast(u32x4, pack8(acc[c], hf)), ors, ob + c * 1024 + hf * 512, 0, 16); };
#pragma unroll
    for (int i = 0; i < 6; ++i) { rdv7(i ^ 1); SB(); }
    static_for<0, 16>([&](auto I) AI {
      constexpr int i = I.value;
      const u32x4 pv7 = {p7[4 * (i & 3)], p7[4 * (i & 3) + 1], p7[4 * (i & 3) + 2], p7[4 * (i & 3) + 3]};
      acc[i >> 2] = MFMA32(FR[i & 7], __builtin_bit_cast(bf16x8, pv7), acc[i >> 2]);
      PIN(acc[i >> 2]);
      SB();
      if constexpr ((i & 1) == 0 && i < 10) { rdv7(i + 7); SB(); rdv7(i + 6); }
      if constexpr (i >= 5 && (i & 1)) sto((i - 5) >> 2, ((i - 5) >> 1) & 1);
      SB();
    });
    STAMP(25);
    sto(3, 0); sto(3, 1);
    SB();
    release();
    STAMP(26);
    wait_vm<0>();
    STAMP(27);
    {
      unsigned old = 0;
      if (lane == 0) old = __hip_atomic_fetch_add((LAS unsigned*)(size_t)(sbase + CTRL + 4 * W_OCNT), 1u, RLX_WG);
      old = __builtin_amdgcn_readfirstlane(old);
      if (old == 2u && lane == 0) __hip_atomic_store((gu64*)(ws + WS_FLAGS) + (size_t)(b * NH + h) * FLAG_STRIDE, tag + TAG_STEP, RLX_AGENT);
      SB();
    }
    wait_word(W_WT);
#pragma unroll
    for (int e = 0; e < 32; ++e) wf[e] = __builtin_amdgcn_raw_buffer_load_b128(ors, wt_off + lane * 16, e * 1024, 16);
    wait_done(NT);
    STAMP(28);
  }
  tag += TAG_STEP;
  const int lane = lane_id();
  {
    gu64* fp = (gu64*)(ws + WS_FLAGS) + (size_t)((rb >> 1) * NH + (ks0 >> 2) + (lane & 1)) * FLAG_STRIDE;
#pragma unroll 1
    for (unsigned sp = 0; sp < SPIN_CAP; ++sp) {
      const u64 f = __hip_atomic_load(fp, RLX_AGENT);
      if (__all(f == tag)) break;
      __builtin_amdgcn_s_sleep(POLL_SLEEP);
    }
    CFENCE();
  }
  STAMP(34);
  f32x4 oa[4][3];
  {
    const unsigned obase = ((rb * 3) * 128 + ks0) * 1024 + lane * 16;
    u32x4 of[8][3];
    auto ldo = [&](int ks) AI {
#pragma unroll
      for (int u = 0; u < 3; ++u) of[ks][u] = __builtin_amdgcn_raw_buffer_load_b128(ors, obase + ks * 1024, u * 131072, 16);
    };
#pragma unroll
    for (int ks = 0; ks < 5; ++ks) ldo(ks);
    SB();
#pragma unroll
    for (int ks = 0; ks < 8; ++ks) {
#pragma unroll
      for (int u = 0; u < 3; ++u)
#pragma unroll
        for (int nb = 0; nb < 4; ++nb) oa[nb][u] = MFMA16(__builtin_bit_cast(bf16x8, wf[ks * 4 + nb]), __builtin_bit_cast(bf16x8, of[ks][u]), ks == 0 ? f32x4{} : oa[nb][u]);
      SB();
      if (ks == 0) { ldo(5); ldo(6); }
      if (ks == 1) ldo(7);
      SB();
    }
  }
  STAMP(35);
  LAS f32x4* red = (LAS f32x4*)(size_t)sbase;
#pragma unroll
  for (int nb = 0; nb < 4; ++nb)
#pragma unroll
    for (int u = 0; u < 3; ++u) red[(wid * 12 + nb * 3 + u) * 64 + lane] = oa[nb][u];
  bar();
  if (wid < 6) {
    const int u = wid >> 1, r8 = lane & 7, g4 = (lane >> 3) & 3, nl = lane >> 5;
    const int ri = (nl * 3 + u) * 64 + g4 * 16 + (wid & 1) * 8 + r8;
    f32x4 s0 = red[ri], s1 = red[ri + 6 * 64];
#pragma unroll
    for (int w = 1; w < 8; ++w) { s0 += red[w * 12 * 64 + ri]; s1 += red[w * 12 * 64 + ri + 6 * 64]; }
    const unsigned stg = sbase + 98304 + wid * 1024;
    *(LAS u32x2*)(size_t)(stg + r8 * 128 + nl * 32 + g4 * 8) = u32x2{cvtpk(s0[0], s0[1]), cvtpk(s0[2], s0[3])};
    *(LAS u32x2*)(size_t)(stg + r8 * 128 + 64 + nl * 32 + g4 * 8) = u32x2{cvtpk(s1[0], s1[1]), cvtpk(s1[2], s1[3])};
    CFENCE();
    GAS s16x2* op = (GAS s16x2*)(out + (size_t)(rb * 48 + wid * 8 + nl) * DM + cb * 64) + (lane & 31);
#pragma unroll
    for (int i = 0; i < 4; ++i) __builtin_amdgcn_global_atomic_fadd_v2bf16(op + i * DM, __builtin_bit_cast(s16x2, *(LAS unsigned*)(size_t)(stg + i * 256 + lane * 4)));
  } else if (wid == 7 && bid == 0) {
    bool ok = false;
#pragma unroll 1
    for (unsigned sp = 0; sp < SPIN_CAP && !ok; ++sp) {
      bool all = true;
#pragma unroll
      for (int i = 0; i < 4; ++i) all &= __hip_atomic_load((gu64*)(ws + WS_FLAGS) + (size_t)(i * 64 + lane) * FLAG_STRIDE, RLX_AGENT) == tag;
      ok = __all(all);
    }
    if (lane == 0) __hip_atomic_store((gu64*)(ws + WS_EPOCH), tag, RLX_AGENT);
  }
}

extern "C" void kernel_launch(void* const* d_in, const int* in_sizes, int n_in, void* d_out, int out_size, void* d_ws, size_t ws_size,
                              hipStream_t stream) {
  (void)hipFuncSetAttribute((const void*)fused_kernel, hipFuncAttributeMaxDynamicSharedMemorySize, LDS_BYTES);
  fused_kernel<<<256, 512, LDS_BYTES, stream>>>((const u16*)d_in[0], (const u16*)d_in[1], (const u16*)d_in[2], (const u16*)d_in[3],
                                                (const u16*)d_in[4], (u16*)d_out, (char*)d_ws);
}
```

```cpp
#include <hip/hip_runtime.h>
#include <hip/hip_bf16.h>
#include <utility>

#define LAS __attribute__((address_space(3)))
#define GAS __attribute__((address_space(1)))
#define DI __device__ __forceinline__
#define AI __attribute__((always_inline))
typedef __bf16 bf16x8 __attribute__((ext_vector_type(8)));
typedef __bf16 bf16x4 __attribute__((ext_vector_type(4)));
typedef __bf16 bf16x2 __attribute__((ext_vector_type(2)));
typedef float f32x16 __attribute__((ext_vector_type(16)));
typedef float f32x4 __attribute__((ext_vector_type(4)));
typedef float f32x2 __attribute__((ext_vector_type(2)));
typedef unsigned u32x4 __attribute__((ext_vector_type(4)));
typedef short s16x2 __attribute__((ext_vector_type(2)));
typedef unsigned short u16;
typedef unsigned long long u64;
typedef GAS u64 gu64;
#define RLX_AGENT __ATOMIC_RELAXED, __HIP_MEMORY_SCOPE_AGENT
#define RLX_WG __ATOMIC_RELAXED, __HIP_MEMORY_SCOPE_WORKGROUP

constexpr int WIN = 2;
constexpr int AUX_LD = 3;
constexpr int AUX_AT = 17;
constexpr int PACE_A = 600, PACE_T = 54, JITTER = 54;
constexpr int THROT = 8;
constexpr unsigned CODE_BYTES = 20252;

constexpr int NB = 8, SQ = 96, DM = 512, NH = 32, SK = 512, DH = 128;
constexpr int SLOTB = 16384, NT = 24;
constexpr int XREG = 6 * SLOTB, XAREA = 12288;
constexpr int CTRL = XREG + 5 * XAREA;
constexpr int SCR2 = CTRL + 1024;
constexpr int DUMP = SCR2 + 2048;
constexpr int LDS_BYTES = 163840;
constexpr int W_FULL = 0, W_DONE = 4, W_OCNT = 8, W_TAG = 12, W_WT = 15;
constexpr size_t WS_EPOCH = 0, WS_FLAGS = 4096, WS_FLAG2 = 40960, WS_O = 65536, O_BYTES = (size_t)NB * 6 * 128 * 1024, WT_BYTES = (size_t)NH * DH * DM * 2;
constexpr int FLAG_STRIDE = 16;
constexpr unsigned SPIN_CAP = 1u << 20;
constexpr u64 TAG_STEP = 0x9E3779B97F4A7C15ull;

constexpr int slot_tab[NT] = {0, 1, 2, 3, 0, 1, 2, 3, 4, 5, 0, 1, 2, 6, 3, 7, 8, 4, 5, 0, 1, 2, 6, 3};
constexpr int need_tab[NT] = {0, 0, 0, 0, 1, 2, 3, 4, 0, 0, 5, 6, 7, 0, 8, 0, 0, 9, 10, 11, 12, 13, 14, 15};
constexpr int tile_at(int n) { return n < 4 ? n : (n < 6 ? n + 4 : (n < 10 ? n - 2 : n)); }
constexpr int order_of(int c) { return c < 4 ? c : (c < 8 ? c + 2 : (c < 10 ? c - 4 : c)); }
constexpr int nops(int n) { return n == 0 ? 10 : (tile_at(n) < 7 ? 7 : 4); }
constexpr int pub_tab[10] = {1, 1, 2, 3, 3, 4, 4, 7, 8, 9};
constexpr int pubto(int n) { return pub_tab[n]; }
constexpr int inflight(int p, int n) { int s = (p == 1 ? 3 : 0); for (int i = p; i <= n; ++i) s += nops(i); return s; }
constexpr u64 pack_slots(int lo) { u64 v = 0; for (int i = 0; i < 16 && lo + i < NT; ++i) v |= (u64)slot_tab[lo + i] << (4 * i); return v; }

#define SB() __builtin_amdgcn_sched_barrier(0)
#define CFENCE() asm volatile("" ::: "memory")
template <int N> DI void wait_vm() { static_assert(N >= 0 && N < 64, "vmcnt"); asm volatile("s_waitcnt vmcnt(%0)" ::"n"(N) : "memory"); }
DI void bar() { asm volatile("s_waitcnt lgkmcnt(0)\n\ts_barrier" ::: "memory"); }
DI unsigned cvtpk(float lo, float hi) { f32x2 v = {lo, hi}; bf16x2 b = __builtin_convertvector(v, bf16x2); return __builtin_bit_cast(unsigned, b); }
DI bf16x8 pack8(const f32x16& x, int s) {
  u32x4 w = {cvtpk(x[8 * s], x[8 * s + 1]), cvtpk(x[8 * s + 2], x[8 * s + 3]), cvtpk(x[8 * s + 4], x[8 * s + 5]), cvtpk(x[8 * s + 6], x[8 * s + 7])};
  return __builtin_bit_cast(bf16x8, w);
}
DI bf16x4 trread(unsigned a) { return __builtin_amdgcn_ds_read_tr16_b64_v4bf16((LAS bf16x4*)(size_t)a); }
DI bf16x8 tr2(unsigned a0, unsigned a1) { bf16x4 lo = trread(a0), hi = trread(a1); return __builtin_shufflevector(lo, hi, 0, 1, 2, 3, 4, 5, 6, 7); }
DI float swap32max(float v) { auto r = __builtin_amdgcn_permlane32_swap(__float_as_uint(v), __float_as_uint(v), false, false); return fmaxf(__uint_as_float(r[0]), __uint_as_float(r[1])); }
DI float swap32sum(float v) { auto r = __builtin_amdgcn_permlane32_swap(__float_as_uint(v), __float_as_uint(v), false, false); return __uint_as_float(r[0]) + __uint_as_float(r[1]); }
DI int lane_id() { int l; asm volatile("v_mbcnt_lo_u32_b32 %0, -1, 0\n\tv_mbcnt_hi_u32_b32 %0, -1, %0" : "=v"(l)); return l; }
DI unsigned opaque(unsigned v) { asm volatile("" : "+v"(v)); return v; }
#define MFMA32(a, b, c) __builtin_amdgcn_mfma_f32_32x32x16_bf16((a), (b), (c), 0, 0, 0)
#define MFMA16(a, b, c) __builtin_amdgcn_mfma_f32_16x16x32_bf16((a), (b), (c), 0, 0, 0)
template <int I, int N, class F> DI void static_for(F&& f) { if constexpr (I < N) { f(std::integral_constant<int, I>{}); static_for<I + 1, N>(f); } }

template <bool NTL> DI void dma4(unsigned lds, const char* g, unsigned v0, unsigned v1, unsigned v2, unsigned v3) {
  unsigned keep;
  if constexpr (NTL)
    asm volatile("s_mov_b32 %0, m0\n\ts_mov_b32 m0, %1\n\ts_nop 3\n\tglobal_load_lds_dwordx4 %3, %2 nt\n\t"
                 "s_add_u32 m0, m0, 0x400\n\ts_nop 0\n\tglobal_load_lds_dwordx4 %4, %2 nt\n\t"
                 "s_add_u32 m0, m0, 0x400\n\ts_nop 0\n\tglobal_load_lds_dwordx4 %5, %2 nt\n\t"
                 "s_add_u32 m0, m0, 0x400\n\ts_nop 0\n\tglobal_load_lds_dwordx4 %6, %2 nt\n\ts_mov_b32 m0, %0"
                 : "=&s"(keep) : "s"(lds), "s"(g), "v"(v0), "v"(v1), "v"(v2), "v"(v3) : "memory", "scc");
  else
    asm volatile("s_mov_b32 %0, m0\n\ts_mov_b32 m0, %1\n\ts_nop 3\n\tglobal_load_lds_dwordx4 %3, %2\n\t"
                 "s_add_u32 m0, m0, 0x400\n\ts_nop 0\n\tglobal_load_lds_dwordx4 %4, %2\n\t"
                 "s_add_u32 m0, m0, 0x400\n\ts_nop 0\n\tglobal_load_lds_dwordx4 %5, %2\n\t"
                 "s_add_u32 m0, m0, 0x400\n\ts_nop 0\n\tglobal_load_lds_dwordx4 %6, %2\n\ts_mov_b32 m0, %0"
                 : "=&s"(keep) : "s"(lds), "s"(g), "v"(v0), "v"(v1), "v"(v2), "v"(v3) : "memory", "scc");
}

DI void touch(unsigned lds, const char* g, unsigned v) {
  unsigned keep;
  asm volatile("s_mov_b32 %0, m0\n\ts_mov_b32 m0, %1\n\ts_nop 3\n\tglobal_load_lds_dword %3, %2\n\ts_mov_b32 m0, %0"
               : "=&s"(keep) : "s"(lds), "s"(g), "v"(v) : "memory");
}
DI void dma3(unsigned lds, const char* g, unsigned v0, unsigned v1, unsigned v2) {
  unsigned keep;
  asm volatile("s_mov_b32 %0, m0\n\ts_mov_b32 m0, %1\n\ts_nop 3\n\tglobal_load_lds_dwordx4 %3, %2\n\t"
               "s_add_u32 m0, m0, 0x400\n\ts_nop 0\n\tglobal_load_lds_dwordx4 %4, %2\n\t"
               "s_add_u32 m0, m0, 0x400\n\ts_nop 0\n\tglobal_load_lds_dwordx4 %5, %2\n\ts_mov_b32 m0, %0"
               : "=&s"(keep) : "s"(lds), "s"(g), "v"(v0), "v"(v1), "v"(v2) : "memory", "scc");
}

__global__ __launch_bounds__(512, 2) void fused_kernel(const u16* __restrict__ x, const u16* __restrict__ kc, const u16* __restrict__ vc,
                                                      const u16* __restrict__ wq, const u16* __restrict__ wo, u16* out, char* ws) {
  extern __shared__ __attribute__((aligned(16))) char smem[];
  const char* pc0;
  asm volatile("s_getpc_b64 %0" : "=s"(pc0) : "s"(x), "s"(kc), "s"(vc), "s"(wq), "s"(wo), "s"(out), "s"(ws));
  const int tid = threadIdx.x;
  const int wid = __builtin_amdgcn_readfirstlane(tid >> 6);
  const int bid = blockIdx.x, xcd = bid & 7, idx = bid >> 3;
  const int h = xcd * 4 + (idx & 3), b = idx >> 2;
  const int kh = xcd & 1, rb = 8 * (xcd >> 2) + (idx >> 2), cb = 4 * ((xcd >> 1) & 1) + (idx & 3);
  const int ks0 = kh * 64 + ((wid + (idx >> 2)) & 7) * 8;
  const int rot = (b + 2 * (idx & 3)) & 7;
  const unsigned sbase = (unsigned)(size_t)(LAS char*)smem;
  volatile LAS unsigned* ctl = (volatile LAS unsigned*)(size_t)(sbase + CTRL);
  u32x4 wf[32];
  u64 tag;
  const __amdgpu_buffer_rsrc_t ors = __builtin_amdgcn_make_buffer_rsrc(ws + WS_O, 0, (int)(O_BYTES + WT_BYTES), 0x00020000);
  const unsigned wt_off = (unsigned)O_BYTES + (unsigned)(cb * 128 + ks0) * 4096;
  unsigned done_seen = 0;
  auto wait_done = [&](unsigned need) AI {
    if (done_seen < need) {
#pragma unroll 1
      for (unsigned sp = 0; sp < SPIN_CAP; ++sp) {
        const u32x4 d = *(volatile LAS u32x4*)(size_t)(sbase + CTRL + 4 * W_DONE);
        done_seen = __builtin_amdgcn_readfirstlane(min(d.x, min(d.y, d.z)));
        if (done_seen >= need) break;
        __builtin_amdgcn_s_sleep(1);
      }
    }
    CFENCE();
  };
  auto wait_word = [&](int w) AI {
#pragma unroll 1
    for (unsigned sp = 0; sp < SPIN_CAP; ++sp) { if (__builtin_amdgcn_readfirstlane(ctl[w]) != 0u) break; __builtin_amdgcn_s_sleep(1); }
    CFENCE();
  };
  auto wait_landed = [&](unsigned n) AI {
#pragma unroll 1
    for (unsigned sp = 0; sp < SPIN_CAP; ++sp) { if ((__builtin_amdgcn_readfirstlane(ctl[W_FULL]) & 255u) >= n) break; __builtin_amdgcn_s_sleep(2); }
    CFENCE();
  };
  auto get_tag = [&]() AI { wait_word(W_TAG + 2); tag = (u64)ctl[W_TAG] | ((u64)ctl[W_TAG + 1] << 32); };

  if (__builtin_expect(wid >= 4, 1)) {
    const int lane = tid & 63, q = 7 - wid;
    if (q == 3) touch(sbase + DUMP, pc0, min((unsigned)(idx + 32 * (lane & 15)) * 64u, CODE_BYTES));
    unsigned tstart = 0;
    const unsigned hs = ((unsigned)bid * 0x9E3779B1u) ^ (((unsigned)bid * 0x85EBCA77u) >> 13);
    const int kvrot = (hs >> 20) & 7, kvmul = (int)((hs >> 9) & 6) | 1, jslot = (hs >> 24) & 31;
    const int r7 = lane >> 3, sl = lane & 7;
    const unsigned c0 = (sl ^ (((r7 & 2) << 1) | (r7 >> 2))) * 16, c1 = c0 ^ 32;
    const unsigned kv0 = r7 * 256 + c0, kv2 = (8 + r7) * 256 + c1, w0 = r7 * 8192 + c0, w2 = (8 + r7) * 8192 + c1;
    const char* kb = (const char*)kc + (size_t)(b * NH + h) * SK * DH * 2 + q * 4096;
    const char* vb = (const char*)vc + (size_t)(b * NH + h) * SK * DH * 2 + q * 4096;
    const char* wb = (const char*)wq + h * DH * 2 + q * 16 * 8192;
    const unsigned ldsq = sbase + q * 4096;
    volatile LAS unsigned char* fullb = (volatile LAS unsigned char*)(size_t)(sbase + CTRL + 4 * W_FULL);
    const char* xb = (const char*)x + (size_t)(b * SQ + q * 24) * (DM * 2);
    const unsigned xc = c0 ^ ((q & 1) * 32), x0 = r7 * 1024 + xc, x1 = (8 + r7) * 1024 + (xc ^ 32), x2 = x0 + 16 * 1024;
    auto ldx = [&](int j) AI { dma3(sbase + XREG + (j % 5) * XAREA + q * 3072, xb + ((j + rot) & 7) * 128, x0, x1, x2); };
    auto request = [&](auto N) AI {
      constexpr int n = N.value, c = tile_at(n);
      if constexpr (need_tab[c] > 0) wait_done(need_tab[c]);
      const unsigned lds = ldsq + slot_tab[c] * SLOTB;
      if constexpr (c < 8) {
        dma4<false>(lds, wb + (size_t)((c + rot) & 7) * 64 * 8192, w0, w0 + 128, w2, w2 + 128);
        if constexpr (c == 0) ldx(0);
        if constexpr (c < 7) ldx(c + 1);
        if constexpr (c == 0) { SB(); tstart = (unsigned)__builtin_amdgcn_s_memrealtime() + (unsigned)(jslot * JITTER) / 32u - JITTER / 2 - 30u; bar(); }
      } else {
        dma4<true>(lds, kb + (((c - 8) * kvmul + kvrot) & 7) * SLOTB, kv0, kv0 + 128, kv2, kv2 + 128);
      }
      if constexpr (pubto(n) > (n ? pubto(n - 1) : 0)) { wait_vm<inflight(pubto(n), n)>(); fullb[q] = (unsigned char)pubto(n); CFENCE(); }
    };
    static_for<0, 10>(request);
    if (q < 2) {
      u64 seq = pack_slots(10);
      unsigned due = tstart + PACE_A;
      const size_t kvd = (size_t)(vb - kb);
#pragma unroll 1
      for (int n = 10; n < NT; ++n) {
        wait_done(n < 13 ? n - 5 : (n == 13 ? 7 : (n < 17 ? 8 : n - 8)));
        if (n >= 12) {
#pragma unroll 1
          while ((int)((unsigned)__builtin_amdgcn_s_memrealtime() - due) < 0) __builtin_amdgcn_s_sleep(2);
        }
        due += PACE_T;
        const bool isk = (n & 1) && n != 23;
        const int ti = isk ? (n - 7) >> 1 : (n - 9) >> 1;
        const char* g = kb + (isk ? (size_t)0 : kvd) + ((ti * kvmul + kvrot) & 7) * SLOTB;
        const unsigned lds = ldsq + ((unsigned)seq & 15u) * SLOTB;
        seq >>= 4;
        dma4<true>(lds, g, kv0, kv0 + 128, kv2, kv2 + 128);
        dma4<true>(lds + 8192, g + 8192, kv0, kv0 + 128, kv2, kv2 + 128);
        if (n == 10) { wait_vm<8>(); fullb[q] = 10; }
        else if (n >= 10 + WIN) { wait_vm<8 * WIN>(); fullb[q] = (unsigned char)(n + 1 - WIN); }
        CFENCE();
      }
      static_for<0, WIN>([&](auto I) AI {
        constexpr int i = I.value;
        wait_vm<8 * (WIN - 1 - i)>(); fullb[q] = (unsigned char)(NT - WIN + i + 1); CFENCE();
      });
      wait_done(NT);
    } else {
      wait_vm<0>();
      fullb[q] = 127;
      CFENCE();
      wait_landed(NT - WIN);
    }
    wait_word(W_WT);
#pragma unroll
    for (int e = 0; e < 32; ++e) {
      wf[e] = __builtin_amdgcn_raw_buffer_load_b128(ors, wt_off + lane * 16, e * 1024, 16);
      if ((e & 3) == 3) { SB(); wait_vm<THROT>(); SB(); }
    }
    wait_done(NT);
    get_tag();
  } else if (wid == 3) {
    const int lane = tid & 63;
    const int ks = bid >> 1, half = bid & 1;
    {
      const char* src = (const char*)wo + (size_t)(ks * 32 + (lane >> 3)) * 1024 + half * 512 + (lane & 7) * 16;
      u32x4 raw[16];
      if (lane < 16) ctl[lane] = 0u;
      bar();
      wait_landed(AUX_LD);
      tag = __hip_atomic_load((gu64*)(ws + WS_EPOCH), RLX_AGENT);
#pragma unroll
      for (int j = 0; j < 4; ++j)
#pragma unroll
        for (int a = 0; a < 4; ++a) raw[4 * j + a] = __builtin_nontemporal_load((const u32x4*)(src + a * 8192 + j * 128));
      SB();
      const int g4 = lane >> 4, qq = (lane & 15) >> 2, p = lane & 3;
      const unsigned sc = sbase + SCR2, ta = sc + 128 * (4 * (g4 & 1) + qq) + 32 * (g4 >> 1) + 8 * p;
      const unsigned dst = (unsigned)O_BYTES + (unsigned)(half * 4 * 128 + ks) * 4096 + (lane >> 5) * 1024 + (lane & 31) * 16;
#pragma unroll
      for (int ps = 0; ps < 8; ++ps) {
        *(LAS u32x4*)(size_t)(sc + lane * 16) = raw[2 * ps];
        *(LAS u32x4*)(size_t)(sc + 1024 + lane * 16) = raw[2 * ps + 1];
        CFENCE();
#pragma unroll
        for (int m = 0; m < 2; ++m) raw[2 * ps + m] = __builtin_bit_cast(u32x4, tr2(ta + m * 64, ta + m * 64 + 1024));
        CFENCE();
      }
#pragma unroll
      for (int e = 0; e < 16; ++e) __builtin_amdgcn_raw_buffer_store_b128(raw[e], ors, dst + ((e >> 1) & 1) * 512 + (e & 1) * 2048, (e >> 2) * 128 * 4096, 16);
      {
        const __amdgpu_buffer_rsrc_t urs = __builtin_amdgcn_make_buffer_rsrc(out, 0, NB * SQ * DM * 2, 0x00020000);
#pragma unroll
        for (int e = 0; e < 3; ++e) __builtin_amdgcn_raw_buffer_store_b128(u32x4{0u, 0u, 0u, 0u}, urs, bid * 3072 + lane * 16, e * 1024, 16);
      }
      ctl[W_TAG] = (unsigned)tag; ctl[W_TAG + 1] = (unsigned)(tag >> 32);
      CFENCE();
      ctl[W_TAG + 2] = 1u;
      wait_vm<0>();
      if (lane == 0) __hip_atomic_store((gu64*)(ws + WS_FLAG2) + half * 128 + ks, tag + TAG_STEP, RLX_AGENT);
    }
    wait_done(AUX_AT);
    touch(sbase + DUMP, pc0, min((unsigned)(idx + 32 * (lane & 15)) * 64u, CODE_BYTES));
    {
      const __amdgpu_buffer_rsrc_t frs = __builtin_amdgcn_make_buffer_rsrc(ws + WS_FLAG2, 0, 2048, 0x00020000);
      const unsigned tl = (unsigned)(tag + TAG_STEP), th = (unsigned)((tag + TAG_STEP) >> 32);
#pragma unroll 1
      for (unsigned sp = 0; sp < SPIN_CAP; ++sp) {
        const u32x4 v = __builtin_amdgcn_raw_buffer_load_b128(frs, lane * 16, 0, 16), w = __builtin_amdgcn_raw_buffer_load_b128(frs, lane * 16, 1024, 16);
        if (__all(v.x == tl && v.y == th && v.z == tl && v.w == th && w.x == tl && w.y == th && w.z == tl && w.w == th)) break;
        __builtin_amdgcn_s_sleep(32);
        CFENCE();
      }
      CFENCE();
      ctl[W_WT] = 1u;
      CFENCE();
      if (bid == 0 && lane == 0) __hip_atomic_store((gu64*)(ws + WS_EPOCH), tag + TAG_STEP, RLX_AGENT);
    }
    wait_landed(NT - WIN);
#pragma unroll
    for (int e = 0; e < 32; ++e) {
      wf[e] = __builtin_amdgcn_raw_buffer_load_b128(ors, wt_off + lane * 16, e * 1024, 16);
      if ((e & 3) == 3) { SB(); wait_vm<THROT>(); SB(); }
    }
    wait_done(NT);
  } else {
    int lane = tid & 63;
    int r = lane & 31, hh = lane >> 5, blk = (lane >> 4) & 1, qq = (lane & 15) >> 2, p = lane & 3;
    const int ps = ((p & 1) << 1) | (p >> 1);
    bar();
    unsigned fl;
    const unsigned cta = opaque(sbase + CTRL), dna = opaque(sbase + CTRL + 4 * (W_DONE + wid));
    unsigned dnv = 0;
    auto lacking = [](unsigned v, int n) AI { return (((unsigned)__builtin_amdgcn_readfirstlane(v) - (unsigned)(n + 1) * 0x01010101u) & 0x80808080u) != 0u; };
    auto peek = [&]() AI { fl = *(volatile LAS unsigned*)(size_t)(cta + 4 * W_FULL); };
    auto wait_full = [&](int n) AI {
      if (__builtin_expect(lacking(fl, n), 0)) {
#pragma unroll 1
        for (unsigned sp = 0; sp < SPIN_CAP; ++sp) { if (!lacking(*(volatile LAS unsigned*)(size_t)(cta + 4 * W_FULL), n)) break; }
      }
      CFENCE();
    };
    auto release = [&]() AI { CFENCE(); dnv += 1; asm volatile("" : "+v"(dnv)); *(volatile LAS unsigned*)(size_t)dna = dnv; CFENCE(); };
    const unsigned wa = sbase + 2048 * hh + 128 * qq + 16 * ((2 * blk + (ps >> 1)) ^ (((qq & 2) << 1) | (2 * hh))) + 8 * (ps & 1);
    const unsigned va = sbase + 128 * (4 * hh + qq) + 16 * ((2 * blk + (p >> 1)) ^ (((qq & 2) << 1) | hh)) + 8 * (p & 1);
    const unsigned fr = 128 * (r & 7) + 16 * (hh ^ (((r & 2) << 1) | ((r >> 2) & 3)));
    const unsigned ka = sbase + 2048 * (r >> 3) + fr;
    const unsigned xa = sbase + XREG + 1024 * (4 * wid + (r >> 3)) + fr;
    f32x16 acc[4];
    bf16x8 FR[8];
    unsigned ab[4], kb4[4];
    auto setab = [&](unsigned (&d)[4], unsigned a, unsigned x1, unsigned x2) AI { d[0] = opaque(a); d[1] = opaque(a ^ x1); d[2] = opaque(a ^ x2); d[3] = opaque(a ^ x1 ^ x2); };
    auto seta = [&](unsigned (&d)[4], unsigned a, int j) AI { d[j] = opaque(j == 0 ? a : d[0] ^ (32 * j)); };
    auto rdw = [&](int i, int so) AI { const int ks = i >> 2, c = i & 3, o = so + 4096 * ks + 1024 * (c >> 1); FR[i & 7] = tr2(ab[2 * (c & 1)] + o, ab[2 * (c & 1) + 1] + o + 512); };
    auto rdv = [&](int i) AI { const int ks = i >> 2, c = i & 3, o = 4096 * ks + 1024 * (c >> 1); FR[i & 7] = tr2(ab[2 * (c & 1)] + o, ab[2 * (c & 1) + 1] + o + 2048); };
    auto rdk = [&](int i) AI { FR[i & 7] = *(LAS bf16x8*)(size_t)(kb4[i & 3] + 1024 * ((i >> 2) & 1) + 8192 * (i >> 3)); };
    auto ready = [&](int c) AI { wait_full(c); SB(); };
    {
      bf16x8 XF[4];
      unsigned xb4[4] = {opaque(xa), opaque(xa ^ 32), opaque(xa ^ 64), opaque(xa ^ 96)};
      auto rdx = [&](int tile, int ks) AI { XF[ks] = *(LAS bf16x8*)(size_t)(xb4[ks] + (tile % 5) * XAREA); };
      setab(ab, wa, 16, 64);
      setab(kb4, ka + slot_tab[8] * SLOTB, 32, 64);
      peek(); wait_full(0);
      rdx(0, 0); rdx(0, 1);
#pragma unroll
      for (int i = 0; i < 6; ++i) { rdw(i ^ 1, 0); SB(); }
      SB();
      static_for<0, 8>([&](auto TL) AI {
        constexpr int tile = TL.value;
        static_for<0, 16>([&](auto I) AI {
          constexpr int i = I.value;
          if constexpr (i == 10) { wait_full(order_of(tile + 1)); SB(); }
          acc[i & 3] = MFMA32(FR[i & 7], XF[i >> 2], (tile == 0 && i < 4) ? f32x16{} : acc[i & 3]);
          SB();
          if constexpr ((i & 1) == 0) {
            if constexpr (i < 10) { rdw(i + 7, (slot_tab[tile] & 3) * SLOTB); SB(); rdw(i + 6, (slot_tab[tile] & 3) * SLOTB); }
            else if constexpr (tile < 7) { rdw(i - 9, (slot_tab[tile + 1] & 3) * SLOTB); SB(); rdw(i - 10, (slot_tab[tile + 1] & 3) * SLOTB); }
            else { rdk(i - 9); SB(); rdk(i - 10); }
          }
          if constexpr (i == 1) rdx(tile, 2);
          if constexpr (i == 5) rdx(tile, 3);
          if constexpr (i == 7) peek();
          if constexpr (i == 9) release();
          if constexpr (i == 11 && tile < 7) rdx(tile + 1, 0);
          if constexpr (i == 13 && tile < 7) rdx(tile + 1, 1);
          SB();
        });
      });
    }
    const float CS = 0.08838834764831845f * 1.4426950408889634f;
    bf16x8 qf[8];
#pragma unroll
    for (int c = 0; c < 4; ++c) { acc[c] *= CS; qf[2 * c] = pack8(acc[c], 0); qf[2 * c + 1] = pack8(acc[c], 1); }
#pragma unroll
    for (int c = 0; c < 4; ++c) acc[c] = f32x16{};
    float l = 0.f, alpha = 1.f, rs0 = 0.f, rs1 = 0.f, mxa, mxb;
    bool pend = false;
    f32x16 s0, s1, negm;
    float e0[16], e1[16];
    unsigned pw[16];
#define PIN(v) asm volatile("" : "+v"(v))
    auto ex0 = [&](int i) AI { e0[i] = __builtin_amdgcn_exp2f(s0[i]); PIN(e0[i]); };
    auto ex1 = [&](int i) AI { e1[i] = __builtin_amdgcn_exp2f(s1[i]); PIN(e1[i]); };
    auto ad0 = [&](int i) AI { rs0 += e0[i]; PIN(rs0); rs1 += e0[i + 1]; PIN(rs1); };
    auto ad1 = [&](int i) AI { rs0 += e1[i]; PIN(rs0); rs1 += e1[i + 1]; PIN(rs1); };
    auto pk0 = [&](int w) AI { pw[w] = cvtpk(e0[2 * w], e0[2 * w + 1]); PIN(pw[w]); };
    auto pk1 = [&](int w) AI { pw[8 + w] = cvtpk(e1[2 * w], e1[2 * w + 1]); PIN(pw[8 + w]); };
    auto pfrag = [&](int k) AI { u32x4 v = {pw[4 * k], pw[4 * k + 1], pw[4 * k + 2], pw[4 * k + 3]}; return __builtin_bit_cast(bf16x8, v); };
    auto mx0 = [&](int i) AI { float& m = (i & 1) ? mxb : mxa; m = i < 2 ? fmaxf(s0[2 * i], s0[2 * i + 1]) : fmaxf(fmaxf(m, s0[2 * i]), s0[2 * i + 1]); PIN(m); };
    auto mx1 = [&](int i) AI { float& m = (i & 1) ? mxb : mxa; m = fmaxf(fmaxf(m, s1[2 * i]), s1[2 * i + 1]); PIN(m); };
    auto shift_ref = [&](float d) AI {
#pragma unroll
      for (int e = 0; e < 16; ++e) { s0[e] -= d; s1[e] -= d; negm[e] -= d; }
    };
    auto decide = [&]() AI {
      const float mx = fmaxf(mxa, mxb);
      if (__builtin_expect(__any(mx > 8.f), 0)) {
        const float d = fmaxf(swap32max(mx), 0.f);
        shift_ref(d);
        alpha = __builtin_amdgcn_exp2f(-d);
        l = (l + rs0 + rs1) * alpha; rs0 = 0.f; rs1 = 0.f;
        pend = true;
      }
      SB();
    };
    auto rescale = [&]() AI {
      if (__builtin_expect(pend, 0)) {
#pragma unroll
        for (int cc = 0; cc < 4; ++cc) acc[cc] *= alpha;
        pend = false;
      }
      SB();
    };
    negm = f32x16{};
    static_for<0, 16>([&](auto I) AI {
      constexpr int i = I.value;
      if constexpr (i == 10) { ready(9); setab(kb4, ka + slot_tab[9] * SLOTB, 32, 64); SB(); }
      if constexpr (i < 8) s0 = MFMA32(FR[i & 7], qf[i & 7], (i == 0 ? negm : s0)); else s1 = MFMA32(FR[i & 7], qf[i & 7], (i == 8 ? negm : s1));
        if constexpr (i < 8) PIN(s0); else PIN(s1);
      SB();
      if constexpr ((i & 1) == 0) { rdk((i + 7) & 15); SB(); rdk((i + 6) & 15); }
      if constexpr (i == 7) peek();
      if constexpr (i == 9) release();
      if constexpr (i >= 11 && i < 15) { mx0(2 * (i - 11)); mx0(2 * (i - 11) + 1); }
      SB();
    });
#pragma unroll
    for (int i = 0; i < 8; ++i) mx1(i);
    shift_ref(swap32max(fmaxf(mxa, mxb)));
#pragma unroll
    for (int i = 0; i < 16; ++i) ex0(i);
#pragma unroll
    for (int i = 0; i < 8; ++i) { ad0(2 * i); pk0(i); }
#pragma unroll
    for (int i = 0; i < 4; ++i) ex1(i);
    SB();
    u64 seq = pack_slots(10);
#pragma unroll 1
    for (int c = 9; c < 23; c += 2) {
      rescale();
      static_for<0, 16>([&](auto I) AI {
        constexpr int i = I.value;
        if constexpr (i == 10) ready(c + 1);
        if constexpr (i < 8) s0 = MFMA32(FR[i & 7], qf[i & 7], (i == 0 ? negm : s0)); else s1 = MFMA32(FR[i & 7], qf[i & 7], (i == 8 ? negm : s1));
        if constexpr (i < 8) PIN(s0); else PIN(s1);
        SB();
        if constexpr ((i & 1) == 0) { if constexpr (i < 10) { rdk(i + 7); SB(); rdk(i + 6); } else { rdv(i - 9); SB(); rdv(i - 10); } }
        if constexpr (i == 0) { ex1(4); pk1(0); seta(ab, va + ((unsigned)seq & 15u) * SLOTB, 0); }
        if constexpr (i == 1) { ex1(5); ex1(6); ad1(0); }
        if constexpr (i == 2) { ex1(7); pk1(1); seta(ab, 0, 1); }
        if constexpr (i == 3) { ex1(8); ex1(9); ad1(2); }
        if constexpr (i == 4) { ex1(10); pk1(2); seta(ab, 0, 2); }
        if constexpr (i == 5) { ex1(11); ex1(12); ad1(4); }
        if constexpr (i == 6) { ex1(13); pk1(3); seta(ab, 0, 3); }
        if constexpr (i == 7) { ex1(14); ex1(15); peek(); }
        if constexpr (i == 8) { ad1(6); pk1(4); }
        if constexpr (i == 9) { release(); }
        if constexpr (i == 10) { pk1(5); }
        if constexpr (i == 11) { mx0(0); mx0(1); ad1(8); }
        if constexpr (i == 12) { pk1(6); }
        if constexpr (i == 13) { mx0(2); mx0(3); ad1(10); mx0(4); mx0(5); }
        if constexpr (i == 14) { pk1(7); }
        if constexpr (i == 15) { mx0(6); mx0(7); ad1(12); ad1(14); }
        SB();
      });
      static_for<0, 16>([&](auto I) AI {
        constexpr int i = I.value;
        if constexpr (i == 10) ready(c == 21 ? 21 : c + 2);
        acc[i & 3] = MFMA32(FR[i & 7], pfrag(i >> 2), acc[i & 3]);
        PIN(acc[i & 3]);
        SB();
        if constexpr ((i & 1) == 0) { if constexpr (i < 10) { rdv(i + 7); SB(); rdv(i + 6); } else { rdk(i - 9); SB(); rdk(i - 10); } }
        if constexpr (i == 0) { seta(kb4, ka + ((unsigned)(seq >> 4) & 15u) * SLOTB, 0); seq >>= 8; }
        if constexpr (i == 1) { mx1(0); mx1(1); mx1(2); mx1(3); mx1(4); mx1(5); }
        if constexpr (i == 2) { mx1(6); mx1(7); }
        if constexpr (i == 3) { decide(); ex0(0); ex0(1); }
        if constexpr (i == 4) { seta(kb4, 0, 1); }
        if constexpr (i == 5) { ex0(2); ex0(3); ad0(0); }
        if constexpr (i == 6) { ex0(4); seta(kb4, 0, 2); }
        if constexpr (i == 7) { ex0(5); ex0(6); ad0(2); peek(); }
        if constexpr (i == 8) { ex0(7); seta(kb4, 0, 3); }
        if constexpr (i == 9) { release(); ex0(8); }
        if constexpr (i == 10) { ex0(9); ad0(4); pk0(0); }
        if constexpr (i == 11) { ex0(10); ex0(11); ad0(6); pk0(1); pk0(2); }
        if constexpr (i == 12) { ex0(12); ex0(13); pk0(3); pk0(4); }
        if constexpr (i == 13) { ex0(14); ex0(15); ad0(8); pk0(5); }
        if constexpr (i == 14) { ex1(0); ex1(1); ad0(10); pk0(6); }
        if constexpr (i == 15) { ex1(2); ex1(3); ad0(12); ad0(14); pk0(7); }
        SB();
      });
    }
    peek();
    setab(kb4, va + slot_tab[23] * SLOTB, 32, 64);
#pragma unroll
    for (int i = 4; i < 16; ++i) ex1(i);
#pragma unroll
    for (int i = 0; i < 8; ++i) { ad1(2 * i); pk1(i); }
    float linv = __builtin_amdgcn_rcpf(swap32sum(l + rs0 + rs1));
    PIN(linv);
    rescale();
    lane = lane_id(); r = lane & 31; hh = lane >> 5;
    const unsigned ob = opaque((((b * 6 + 2 * wid + (r >> 4)) * 128 + h * 4) * 64 + hh * 16 + (r & 15)) * 16);
    wait_full(23);
    auto rdv7 = [&](int i) AI { const int ks = i & 3, c = i >> 2, o = 4096 * ks + 1024 * (c >> 1); FR[i & 7] = tr2(kb4[2 * (c & 1)] + o, kb4[2 * (c & 1) + 1] + o + 2048); };
    auto sto = [&](int c, int hf) AI {
      u32x4 w;
#pragma unroll
      for (int e = 0; e < 4; ++e) w[e] = cvtpk(acc[c][8 * hf + 2 * e] * linv, acc[c][8 * hf + 2 * e + 1] * linv);
      __builtin_amdgcn_raw_buffer_store_b128(w, ors, ob + c * 1024 + hf * 512, 0, 16);
    };
#pragma unroll
    for (int i = 0; i < 6; ++i) { rdv7(i ^ 1); SB(); }
    static_for<0, 16>([&](auto I) AI {
      constexpr int i = I.value;
      acc[i >> 2] = MFMA32(FR[i & 7], pfrag(i & 3), acc[i >> 2]);
      PIN(acc[i >> 2]);
      SB();
      if constexpr ((i & 1) == 0 && i < 10) { rdv7(i + 7); SB(); rdv7(i + 6); }
      if constexpr (i >= 5 && (i & 1)) sto((i - 5) >> 2, ((i - 5) >> 1) & 1);
      SB();
    });
    sto(3, 0); sto(3, 1);
    SB();
    release();
    get_tag();
    wait_vm<0>();
    {
      unsigned old = 0;
      if (lane == 0) old = __hip_atomic_fetch_add((LAS unsigned*)(size_t)(sbase + CTRL + 4 * W_OCNT), 1u, RLX_WG);
      old = __builtin_amdgcn_readfirstlane(old);
      if (old == 2u && lane == 0) __hip_atomic_store((gu64*)(ws + WS_FLAGS) + (size_t)(b * NH + h) * FLAG_STRIDE, tag + TAG_STEP, RLX_AGENT);
      SB();
    }
    wait_word(W_WT);
#pragma unroll
    for (int e = 0; e < 32; ++e) wf[e] = __builtin_amdgcn_raw_buffer_load_b128(ors, wt_off + lane * 16, e * 1024, 16);
    wait_done(NT);
  }
  tag += TAG_STEP;
  const int lane = lane_id();
  {
    gu64* fp = (gu64*)(ws + WS_FLAGS) + (size_t)((rb >> 1) * NH + (ks0 >> 2) + (lane & 1)) * FLAG_STRIDE;
#pragma unroll 1
    for (unsigned sp = 0; sp < SPIN_CAP; ++sp) {
      const u64 f = __hip_atomic_load(fp, RLX_AGENT);
      if (__all(f == tag)) break;
      __builtin_amdgcn_s_sleep(1);
    }
    CFENCE();
  }
  constexpr int RP = 272, PT = 48 * RP;
  f32x4 oa[4][3];
  {
    const unsigned obase = ((rb * 3) * 128 + ks0) * 1024 + lane * 16;
    u32x4 of[8][3];
    auto ldo = [&](int ks) AI {
#pragma unroll
      for (int u = 0; u < 3; ++u) of[ks][u] = __builtin_amdgcn_raw_buffer_load_b128(ors, obase + ks * 1024, u * 131072, 16);
    };
#pragma unroll
    for (int ks = 0; ks < 5; ++ks) ldo(ks);
    SB();
#pragma unroll
    for (int ks = 0; ks < 7; ++ks) {
#pragma unroll
      for (int u = 0; u < 3; ++u)
#pragma unroll
        for (int nb = 0; nb < 4; ++nb) oa[nb][u] = MFMA16(__builtin_bit_cast(bf16x8, wf[ks * 4 + nb]), __builtin_bit_cast(bf16x8, of[ks][u]), ks == 0 ? f32x4{} : oa[nb][u]);
      SB();
      if (ks == 0) { ldo(5); ldo(6); }
      if (ks == 1) ldo(7);
      SB();
    }
    const unsigned wa_ = opaque(sbase + wid * PT + (lane & 15) * RP + (lane >> 4) * 16);
#pragma unroll
    for (int t = 0; t < 15; ++t) {
      if (t < 12) oa[t & 3][t >> 2] = MFMA16(__builtin_bit_cast(bf16x8, wf[28 + (t & 3)]), __builtin_bit_cast(bf16x8, of[7][t >> 2]), oa[t & 3][t >> 2]);
      SB();
      if (t >= 3) *(LAS f32x4*)(size_t)(wa_ + ((t - 3) >> 2) * 16 * RP + ((t - 3) & 3) * 64) = oa[(t - 3) & 3][(t - 3) >> 2];
      SB();
    }
  }
  bar();
  {
    const int row = wid * 6 + (lane >> 5);
    const unsigned ra = opaque(sbase + row * RP + (lane & 31) * 8), rb4 = opaque(ra + 4 * PT);
    GAS s16x2* op = (GAS s16x2*)(out + (size_t)(rb * 48 + row) * DM + cb * 64) + (lane & 31);
    f32x2 v[3][8];
#pragma unroll
    for (int i = 0; i < 3; ++i)
#pragma unroll
      for (int w = 0; w < 8; ++w) v[i][w] = *(LAS f32x2*)(size_t)((w < 4 ? ra : rb4) + (w & 3) * PT + i * 2 * RP);
#pragma unroll
    for (int i = 0; i < 3; ++i) {
      const f32x2 t = ((v[i][0] + v[i][1]) + (v[i][2] + v[i][3])) + ((v[i][4] + v[i][5]) + (v[i][6] + v[i][7]));
      __builtin_amdgcn_global_atomic_fadd_v2bf16(op + i * DM, __builtin_bit_cast(s16x2, cvtpk(t[0], t[1])));
    }
  }
}

extern "C" void kernel_launch(void* const* d_in, const int* in_sizes, int n_in, void* d_out, int out_size, void* d_ws, size_t ws_size,
                              hipStream_t stream) {
  (void)hipFuncSetAttribute((const void*)fused_kernel, hipFuncAttributeMaxDynamicSharedMemorySize, LDS_BYTES);
  fused_kernel<<<256, 512, LDS_BYTES, stream>>>((const u16*)d_in[0], (const u16*)d_in[1], (const u16*)d_in[2], (const u16*)d_in[3],
                                                (const u16*)d_in[4], (u16*)d_out, (char*)d_ws);
}
```
